```python
import torch

def _softmax(x, axis=-1):
    return torch.softmax(x, dim=axis)

def _gelu(x):
    return torch.nn.functional.gelu(x, approximate='tanh')

def _silu(x):
    return torch.nn.functional.silu(x)

def _sigmoid(x):
    return torch.sigmoid(x)

def _layernorm(x, axis=-1, eps=1e-05):
    m = x.mean(dim=axis, keepdim=True)
    v = x.var(dim=axis, keepdim=True, unbiased=False)
    return (x - m) / torch.sqrt(v + eps)

def _scatter_add(tgt, idx, src, axis=0):
    return tgt.clone().index_add_(axis, idx, src)

def _segment_sum(src, seg, n):
    out = torch.zeros((n,) + src.shape[1:], dtype=src.dtype)
    return out.index_add_(0, seg, src)

def _pad(x, pw):
    flat = tuple((v for pair in reversed(pw) for v in pair))
    return torch.nn.functional.pad(x, flat)
kernel_name = 'adaLN_cond__gqa_block_cached__geglu_ffn__decode'

def setup_inputs():

    _w = lambda a: (lambda t: t.to(torch.bfloat16) if t.is_floating_point() else t)(torch.as_tensor(a))

    def _A_setup():
        B, Sq, D = (4, 1, 512)
        Hq, Hkv, Sk, Dh = (32, 8, 8192, 128)
        assert Hq % Hkv == 0, (Hq, Hkv)
        return {'x': _w((lambda *s: torch.randn(*s, dtype=torch.bfloat16) * 0.1)(B, Sq, D)), 'k_cache': _w((lambda *s: torch.randn(*s, dtype=torch.bfloat16) * 0.8)(B, Hkv, Sk, Dh)), 'v_cache': _w((lambda *s: torch.randn(*s, dtype=torch.bfloat16) * 1.6)(B, Hkv, Sk, Dh)), 'wq': _w((lambda *s: torch.randn(*s, dtype=torch.bfloat16) * 0.8)(D, Hq * Dh) * D ** (-0.5)), 'wo': _w((lambda *s: torch.randn(*s, dtype=torch.bfloat16) * 1.6)(Hq * Dh, D) * (Hq * Dh) ** (-0.5))}
    _A_in = _A_setup()

    def _B_setup():
        B, S, D, F = (4, 1, 512, 1792)
        return {'x': _w((lambda *s: torch.randn(*s, dtype=torch.bfloat16) * 0.1)(B, S, D)), 'w_gate': _w((lambda *s: torch.randn(*s, dtype=torch.bfloat16) * 0.1)(D, F)), 'w_up': _w((lambda *s: torch.randn(*s, dtype=torch.bfloat16) * 0.1)(D, F)), 'w_down': _w((lambda *s: torch.randn(*s, dtype=torch.bfloat16) * 0.1)(F, D))}
    _B_in = _B_setup()
    return {'A_x': _A_in['x'], 'A_k_cache': _A_in['k_cache'], 'A_v_cache': _A_in['v_cache'], 'A_wq': _A_in['wq'], 'A_wo': _A_in['wo'], 'B_x': _B_in['x'], 'B_w_gate': _B_in['w_gate'], 'B_w_up': _B_in['w_up'], 'B_w_down': _B_in['w_down'], 'c': _w((lambda *s: torch.randn(*s, dtype=torch.bfloat16) * 0.8)(4, 512) * 8.0), 'w_mod': _w((lambda *s: torch.randn(*s, dtype=torch.bfloat16) * 0.8)(512, 6 * 512) * 512 ** (-0.5) * 8.0)}

def reference(A_x, A_k_cache, A_v_cache, A_wq, A_wo, B_x, B_w_gate, B_w_up, B_w_down, c, w_mod):

    def _A_ref(x, k_cache, v_cache, wq, wo):
        B, Sq, D = x.shape
        Hq, Dh = (32, 128)
        Hkv = k_cache.shape[1]
        rep = Hq // Hkv
        q = (lambda x, *d: x.permute(*d))((x @ wq).reshape(B, Sq, Hq, Dh), 0, 2, 1, 3)
        k = (lambda x, n, axis: torch.repeat_interleave(x, n, dim=axis))(k_cache, rep, axis=1)
        v = (lambda x, n, axis: torch.repeat_interleave(x, n, dim=axis))(v_cache, rep, axis=1)
        scores = torch.einsum('bhqd,bhkd->bhqk', q, k) * Dh ** (-0.5)
        p = _softmax(scores, axis=-1)
        o = torch.einsum('bhqk,bhkd->bhqd', p, v)
        return (lambda x, *d: x.permute(*d))(o, 0, 2, 1, 3).reshape(B, Sq, Hq * Dh) @ wo

    def _B_ref(x, w_gate, w_up, w_down):
        return _gelu(x @ w_gate) * (x @ w_up) @ w_down
    x = A_x
    mod = (_silu(c) @ w_mod).reshape(c.shape[0], 6, 1, -1)
    sa, ca, ga, sb, cb, gb = (mod[:, 0].clone(), mod[:, 1].clone(), mod[:, 2].clone(), mod[:, 3].clone(), mod[:, 4].clone(), mod[:, 5].clone())
    h = _layernorm(x, axis=-1) * (1 + ca) + sa
    x = x + ga * _A_ref(h, A_k_cache, A_v_cache, A_wq, A_wo)
    h = _layernorm(x, axis=-1) * (1 + cb) + sb
    x = x + gb * _B_ref(h, B_w_gate, B_w_up, B_w_down)
    return x
```

```cpp
#include <hip/hip_runtime.h>
#include <hip/hip_bf16.h>
#include <stdint.h>

typedef unsigned short u16;
typedef unsigned v4u __attribute__((ext_vector_type(4)));
#define MIMIC 1

constexpr int B = 4, D = 512, HQ = 32, HKV = 8, SK = 8192, DH = 128, F = 1792;

__device__ __forceinline__ float bf2f(u16 b) { return __builtin_bit_cast(float, (unsigned)b << 16); }
__device__ __forceinline__ u16 f2bf(float f) { unsigned u = __builtin_bit_cast(unsigned, f); u += 0x7FFFu + ((u >> 16) & 1u); return (u16)(u >> 16); }
__device__ __forceinline__ float rbf(float f) { return bf2f(f2bf(f)); }

__global__ void k_mod(const u16* c, const u16* wmod, float* mod) {
  __shared__ float sc[B][D];
  for (int i = threadIdx.x; i < B * D; i += blockDim.x) { float v = bf2f(c[i]); sc[i / D][i % D] = rbf(v / (1.f + __expf(-v))); }
  __syncthreads();
  int j = blockIdx.x * blockDim.x + threadIdx.x;
  float a[B] = {0, 0, 0, 0};
  for (int k = 0; k < D; ++k) { float w = bf2f(wmod[(size_t)k * 3072 + j]); for (int b = 0; b < B; ++b) a[b] += sc[b][k] * w; }
  for (int b = 0; b < B; ++b) mod[b * 3072 + j] = rbf(a[b]);
}
__device__ void layernorm_mod(const float* x, const float* cc, const float* ss, float* h, float* red) {
  int t = threadIdx.x;
  float s = 0; for (int i = t; i < D; i += blockDim.x) s += x[i];
  for (int o = 32; o; o >>= 1) s += __shfl_xor(s, o);
  if ((t & 63) == 0) red[t >> 6] = s; __syncthreads();
  s = 0; for (int w = 0; w < (int)blockDim.x / 64; ++w) s += red[w]; __syncthreads();
  float m = rbf(s / D);
  float v = 0; float mf = s / D; for (int i = t; i < D; i += blockDim.x) { float d = x[i] - mf; v += d * d; }
  for (int o = 32; o; o >>= 1) v += __shfl_xor(v, o);
  if ((t & 63) == 0) red[t >> 6] = v; __syncthreads();
  v = 0; for (int w = 0; w < (int)blockDim.x / 64; ++w) v += red[w]; __syncthreads();
  v = rbf(v / D);
  float den = rbf(sqrtf(rbf(v + 1e-5f)));
  for (int i = t; i < D; i += blockDim.x) { float n = rbf(rbf(x[i] - m) / den); h[i] = rbf(rbf(n * rbf(1.f + cc[i])) + ss[i]); }
  __syncthreads();
}
__global__ void k_q(const u16* x, const float* mod, const u16* wq, float* q) {
  __shared__ float xs[D], h[D], red[8];
  int b = blockIdx.y;
  for (int i = threadIdx.x; i < D; i += blockDim.x) xs[i] = bf2f(x[b * D + i]);
  __syncthreads();
  layernorm_mod(xs, mod + b * 3072 + 512, mod + b * 3072, h, red);
  int j = blockIdx.x * blockDim.x + threadIdx.x;
  float a = 0; for (int k = 0; k < D; ++k) a += h[k] * bf2f(wq[(size_t)k * 4096 + j]);
  q[b * 4096 + j] = rbf(a);
}
constexpr int NS = 8;
__global__ void __launch_bounds__(256) k_attn(const float* q, const u16* kc, const u16* vc, float* po, float* pml) {
  __shared__ float qs[4][DH]; __shared__ float sc[4][1024]; __shared__ float red[4][4]; __shared__ float ored[2][4][DH];
  int s = blockIdx.x, hk = blockIdx.y, b = blockIdx.z, t = threadIdx.x;
  for (int i = t; i < 512; i += 256) qs[i / DH][i % DH] = q[b * 4096 + hk * 512 + i];
  __syncthreads();
  const u16* K = kc + ((size_t)(b * HKV + hk) * SK + s * 1024) * DH;
  const u16* V = vc + ((size_t)(b * HKV + hk) * SK + s * 1024) * DH;
  for (int key = t; key < 1024; key += 256) {
    float a[4] = {0, 0, 0, 0};
    _Pragma("unroll 2") for (int d = 0; d < DH; d += 8) { v4u w = *(const v4u*)(K + (size_t)key * DH + d);
      for (int e = 0; e < 4; ++e) { float lo = __builtin_bit_cast(float, w[e] << 16), hi = __builtin_bit_cast(float, w[e] & 0xffff0000u);
        for (int h = 0; h < 4; ++h) a[h] += qs[h][d + 2 * e] * lo + qs[h][d + 2 * e + 1] * hi; } }
    for (int h = 0; h < 4; ++h) { float v = a[h];
      v = rbf(rbf(v) * 0.08838834764831845f);
      sc[h][key] = v; }
  }
  __syncthreads();
  float m[4], l[4];
  for (int h = 0; h < 4; ++h) { float mm = -INFINITY; for (int k = t; k < 1024; k += 256) mm = fmaxf(mm, sc[h][k]);
    for (int o = 32; o; o >>= 1) mm = fmaxf(mm, __shfl_xor(mm, o)); if ((t & 63) == 0) red[h][t >> 6] = mm; }
  __syncthreads();
  for (int h = 0; h < 4; ++h) m[h] = fmaxf(fmaxf(red[h][0], red[h][1]), fmaxf(red[h][2], red[h][3]));
  __syncthreads();
  for (int h = 0; h < 4; ++h) { float ss = 0; for (int k = t; k < 1024; k += 256) { float p = __expf(sc[h][k] - m[h]); sc[h][k] = p; ss += p; }
    for (int o = 32; o; o >>= 1) ss += __shfl_xor(ss, o); if ((t & 63) == 0) red[h][t >> 6] = ss; }
  __syncthreads();
  for (int h = 0; h < 4; ++h) l[h] = red[h][0] + red[h][1] + red[h][2] + red[h][3];
  int d = t & 127, half = t >> 7; float o[4] = {0, 0, 0, 0};
  _Pragma("unroll 4") for (int k = half * 512; k < half * 512 + 512; ++k) { float v = bf2f(V[(size_t)k * DH + d]); for (int h = 0; h < 4; ++h) o[h] += sc[h][k] * v; }
  for (int h = 0; h < 4; ++h) ored[half][h][d] = o[h];
  __syncthreads();
  for (int i = t; i < 512; i += 256) { int h = i / DH, dd = i % DH; po[(((size_t)b * HQ + hk * 4 + h) * NS + s) * DH + dd] = ored[0][h][dd] + ored[1][h][dd]; }
  if (t < 4) { pml[((b * HQ + hk * 4 + t) * NS + s) * 2] = m[t]; pml[((b * HQ + hk * 4 + t) * NS + s) * 2 + 1] = l[t]; }
}
__global__ void k_comb(const float* po, const float* pml, float* o) {
  int h = blockIdx.x, b = blockIdx.y, d = threadIdx.x; const float* ml = pml + (b * HQ + h) * NS * 2;
  float M = -INFINITY; for (int s = 0; s < NS; ++s) M = fmaxf(M, ml[2 * s]);
  float L = 0, a = 0; for (int s = 0; s < NS; ++s) { float w = __expf(ml[2 * s] - M); L += w * ml[2 * s + 1]; a += w * po[(((size_t)b * HQ + h) * NS + s) * DH + d]; }
  o[b * 4096 + h * DH + d] = rbf(a / L);
}
__global__ void k_wo(const float* o, const u16* wo, const u16* x, const float* mod, float* x1) {
  __shared__ float os[4096]; int b = blockIdx.y; for (int i = threadIdx.x; i < 4096; i += blockDim.x) os[i] = o[b * 4096 + i]; __syncthreads();
  int j = blockIdx.x * blockDim.x + threadIdx.x; float a = 0; for (int k = 0; k < 4096; ++k) a += os[k] * bf2f(wo[(size_t)k * D + j]);
  x1[b * D + j] = rbf(bf2f(x[b * D + j]) + rbf(mod[b * 3072 + 1024 + j] * rbf(a)));
}
__global__ void k_ffn1(const float* x1, const float* mod, const u16* wg, const u16* wu, float* g) {
  __shared__ float xs[D], h[D], red[8]; int b = blockIdx.y;
  for (int i = threadIdx.x; i < D; i += blockDim.x) xs[i] = x1[b * D + i]; __syncthreads();
  layernorm_mod(xs, mod + b * 3072 + 2048, mod + b * 3072 + 1536, h, red);
  int j = blockIdx.x * blockDim.x + threadIdx.x; float a = 0, u = 0;
  for (int k = 0; k < D; ++k) { a += h[k] * bf2f(wg[(size_t)k * F + j]); u += h[k] * bf2f(wu[(size_t)k * F + j]); }
  a = rbf(a); u = rbf(u);
  float t = 0.7978845608028654f * (a + 0.044715f * a * a * a); float ge = rbf(0.5f * a * (1.f + tanhf(t)));
  g[b * F + j] = rbf(ge * u);
}
__global__ void k_ffn2(const float* g, const u16* wd, const float* x1, const float* mod, u16* out) {
  __shared__ float gs[F]; int b = blockIdx.y; for (int i = threadIdx.x; i < F; i += blockDim.x) gs[i] = g[b * F + i]; __syncthreads();
  int j = blockIdx.x * blockDim.x + threadIdx.x; float a = 0; for (int k = 0; k < F; ++k) a += gs[k] * bf2f(wd[(size_t)k * D + j]);
  out[b * D + j] = f2bf(x1[b * D + j] + rbf(mod[b * 3072 + 2560 + j] * rbf(a)));
}

extern "C" void kernel_launch(void* const* d_in, const int* in_sizes, int n_in, void* d_out, int out_size, void* d_ws, size_t ws_size, hipStream_t stream) {
  const u16* x = (const u16*)d_in[0]; const u16* kc = (const u16*)d_in[1]; const u16* vc = (const u16*)d_in[2];
  const u16* wq = (const u16*)d_in[3]; const u16* wo = (const u16*)d_in[4];
  const u16* wg = (const u16*)d_in[6]; const u16* wu = (const u16*)d_in[7]; const u16* wd = (const u16*)d_in[8];
  const u16* c = (const u16*)d_in[9]; const u16* wmod = (const u16*)d_in[10];
  float* ws = (float*)d_ws;
  float* mod = ws; float* q = mod + B * 3072; float* po = q + B * 4096; float* pml = po + B * HQ * NS * DH; float* o = pml + B * HQ * NS * 2;
  float* x1 = o + B * 4096; float* g = x1 + B * D;
  k_mod<<<3072 / 64, 64, 0, stream>>>(c, wmod, mod);
  k_q<<<dim3(4096 / 256, B), 256, 0, stream>>>(x, mod, wq, q);
  k_attn<<<dim3(NS, HKV, B), 256, 0, stream>>>(q, kc, vc, po, pml);
  k_comb<<<dim3(HQ, B), 128, 0, stream>>>(po, pml, o);
  k_wo<<<dim3(8, B), 64, 0, stream>>>(o, wo, x, mod, x1);
  k_ffn1<<<dim3(F / 256, B), 256, 0, stream>>>(x1, mod, wg, wu, g);
  k_ffn2<<<dim3(8, B), 64, 0, stream>>>(g, wd, x1, mod, (u16*)d_out);
}
```

```cpp
#include <hip/hip_runtime.h>
#include <hip/hip_bf16.h>
#include <stdint.h>
#include <type_traits>

namespace mk {

typedef unsigned short u16;
typedef unsigned long long u64;
typedef unsigned v4u __attribute__((ext_vector_type(4)));
typedef unsigned v2u __attribute__((ext_vector_type(2)));
typedef float f32x4 __attribute__((ext_vector_type(4)));
typedef float f32x2 __attribute__((ext_vector_type(2)));
typedef short s16x4 __attribute__((ext_vector_type(4)));
typedef __bf16 bf16x8 __attribute__((ext_vector_type(8)));
typedef __bf16 bf16x2 __attribute__((ext_vector_type(2)));
#define LAS __attribute__((address_space(3)))
#define GAS __attribute__((address_space(1)))

#define MK_WIN 3
#define MK_D1 24
#define MK_D2 12
#define MK_D3 12
#define MK_D4 14
#define MK_D5 14
#define MK_DQ2 64
#define MK_W7 8
#define MK_DQ 10
#define MK_DM 32
#define MK_HNT 1
#define MK_WARM 1
#define MK_M1HOLD 1
#define MK_NOQWAIT 0
#define MK_ABL 0
#define MK_DP 8
#define MK_STAMPS 0

constexpr int NB = 4, DM = 512, HQ = 32, HKV = 8, SK = 8192, DH = 128, FF = 1792;
constexpr int NBLK = 256, NTHR = 576;
constexpr unsigned SPIN_MAX = 1u << 17;

constexpr unsigned WS_W = 0;
constexpr unsigned WS_MT = 4096;
constexpr unsigned WS_QT = WS_MT + 49152;
constexpr unsigned WS_XT = WS_QT + 65536;
constexpr unsigned WS_BLK = WS_XT + 8192;
constexpr unsigned BLK_OP = 0;
constexpr unsigned BLK_AP = 2048;
constexpr unsigned BLK_PP = 4096;
constexpr unsigned BLK_YP = 6144;
constexpr unsigned BLK_ML = 8192;
constexpr unsigned BLK_STRIDE = 8320;
constexpr unsigned WS_END = WS_BLK + NBLK * BLK_STRIDE;
constexpr unsigned WS_STAMP = 8u << 20;

constexpr unsigned L_K = 0;
constexpr unsigned L_HW2 = 114688;
constexpr unsigned L_ACT = 131072 + 16384;
constexpr unsigned L_XN = L_ACT + 4096;
constexpr unsigned L_QS = L_XN + 4096;
constexpr unsigned L_C1 = L_QS + 1024;
constexpr unsigned L_S2 = L_C1 + 1024;
constexpr unsigned L_MW = L_S2 + 1024;
constexpr unsigned L_LW = L_MW + 128;
constexpr unsigned L_OB = L_LW + 128;
constexpr unsigned L_H2 = L_OB + 512;
constexpr unsigned L_GB = L_H2 + 512;
constexpr unsigned L_TAG = L_GB + 256;
constexpr unsigned L_FLAG = L_TAG + 16;
constexpr unsigned L_END = L_FLAG + 16;
static_assert(L_END <= 163840, "LDS");
constexpr unsigned R_WO = 0;
constexpr unsigned R_G = 2048;
constexpr unsigned R_U = 3072;
constexpr unsigned R_DN = 4096;
constexpr unsigned R_OM = 8192;

struct Params {
  const u16 *x, *kc, *vc, *wq, *wo, *wg, *wu, *wd, *c, *wmod;
  u16* out;
  char* ws;
};

template <int I, int N, class F> __device__ __forceinline__ void static_for(F&& f) {
  if constexpr (I < N) { f(std::integral_constant<int, I>{}); static_for<I + 1, N>(f); }
}
__device__ __forceinline__ float bf_lo(unsigned u) { return __builtin_bit_cast(float, u << 16); }
__device__ __forceinline__ float bf_hi(unsigned u) { return __builtin_bit_cast(float, u & 0xffff0000u); }
__device__ __forceinline__ unsigned pk2(float a, float b) { f32x2 f = {a, b}; return __builtin_bit_cast(unsigned, __builtin_convertvector(f, bf16x2)); }
__device__ __forceinline__ float rbf(float a) { return bf_lo(pk2(a, a)); }
__device__ __forceinline__ float asf(unsigned u) { return __builtin_bit_cast(float, u); }
__device__ __forceinline__ unsigned asu(float f) { return __builtin_bit_cast(unsigned, f); }
template <int CTRL> __device__ __forceinline__ float dpp(float x) { return asf((unsigned)__builtin_amdgcn_mov_dpp((int)asu(x), CTRL, 0xf, 0xf, true)); }
template <int CTRL> __device__ __forceinline__ unsigned dppu(unsigned x) { return (unsigned)__builtin_amdgcn_mov_dpp((int)x, CTRL, 0xf, 0xf, true); }
constexpr int XOR1 = 0xB1, XOR2 = 0x4E, HMIR = 0x141, ROR4 = 0x124, ROR8 = 0x128;
__device__ __forceinline__ float x16_max(float x) {
  auto s = __builtin_amdgcn_permlane16_swap(asu(x), asu(x), false, false); x = fmaxf(asf(s[0]), asf(s[1]));
  auto t = __builtin_amdgcn_permlane32_swap(asu(x), asu(x), false, false); return fmaxf(asf(t[0]), asf(t[1]));
}
__device__ __forceinline__ float x16_sum(float x) {
  auto s = __builtin_amdgcn_permlane16_swap(asu(x), asu(x), false, false); x = asf(s[0]) + asf(s[1]);
  auto t = __builtin_amdgcn_permlane32_swap(asu(x), asu(x), false, false); return asf(t[0]) + asf(t[1]);
}
__device__ __forceinline__ float row_sum(float x) {
  x += dpp<XOR1>(x); x += dpp<XOR2>(x); x += dpp<ROR4>(x); x += dpp<ROR8>(x); return x;
}
__device__ __forceinline__ float wave_sum(float x) { return x16_sum(row_sum(x)); }
__device__ __forceinline__ f32x4 mfma32(v4u a, v4u b, f32x4 c) {
  return __builtin_amdgcn_mfma_f32_16x16x32_bf16(__builtin_bit_cast(bf16x8, a), __builtin_bit_cast(bf16x8, b), c, 0, 0, 0);
}
__device__ __forceinline__ f32x4 mfma16(v2u a, v2u b, f32x4 c) {
  return __builtin_amdgcn_mfma_f32_16x16x16bf16_1k(__builtin_bit_cast(s16x4, a), __builtin_bit_cast(s16x4, b), c, 0, 0, 0);
}
__device__ __forceinline__ float dot2(unsigned a, unsigned b, float c) {
  return __builtin_amdgcn_fdot2_f32_bf16(__builtin_bit_cast(bf16x2, a), __builtin_bit_cast(bf16x2, b), c, false);
}
__device__ __forceinline__ v2u tr_read(LAS char* p) { return __builtin_bit_cast(v2u, __builtin_amdgcn_ds_read_tr16_b64_v4i16((LAS s16x4*)p)); }
template <int N> __device__ __forceinline__ void wait_vm() { asm volatile("s_waitcnt vmcnt(%0)" ::"i"(N) : "memory"); }
__device__ __forceinline__ void wait_lgkm0() { asm volatile("s_waitcnt lgkmcnt(0)" ::: "memory"); }
__device__ __forceinline__ void bar() { asm volatile("s_waitcnt lgkmcnt(0)\n\ts_barrier" ::: "memory"); }
template <int NT> __device__ __forceinline__ void dma16(const void* base, unsigned voff, unsigned lds) {
  unsigned keep;
  if constexpr (NT)
    asm volatile("s_mov_b32 %0, m0\n\ts_mov_b32 m0, %3\n\ts_nop 0\n\tglobal_load_lds_dwordx4 %1, %2 nt\n\ts_mov_b32 m0, %0" : "=&s"(keep) : "v"(voff), "s"(base), "s"(lds) : "memory");
  else
    asm volatile("s_mov_b32 %0, m0\n\ts_mov_b32 m0, %3\n\ts_nop 0\n\tglobal_load_lds_dwordx4 %1, %2\n\ts_mov_b32 m0, %0" : "=&s"(keep) : "v"(voff), "s"(base), "s"(lds) : "memory");
}
template <int OFF> __device__ __forceinline__ void gload_nt(v4u& r, const void* base, unsigned voff) {
  asm volatile("global_load_dwordx4 %0, %1, %2 offset:%3 nt" : "=v"(r) : "v"(voff), "s"(base), "i"(OFF) : "memory");
}
__device__ __forceinline__ void gload(v4u& r, const void* base, unsigned voff) {
  asm volatile("global_load_dwordx4 %0, %1, %2" : "=v"(r) : "v"(voff), "s"(base) : "memory");
}
__device__ __forceinline__ void pin(v4u& r) { asm volatile("" : "+v"(r)); }
__device__ __forceinline__ void dma16_flat(const void* src, unsigned lds) {
  unsigned keep;
  asm volatile("s_mov_b32 %0, m0\n\ts_mov_b32 m0, %2\n\ts_nop 0\n\tglobal_load_lds_dwordx4 %1, off\n\ts_mov_b32 m0, %0" : "=&s"(keep) : "v"(src), "s"(lds) : "memory");
}
template <int N> __device__ __forceinline__ void wait_vm_on(v4u& r) { asm volatile("s_waitcnt vmcnt(%1)" : "+v"(r) : "i"(N) : "memory"); }
__device__ __forceinline__ void sleep_n(int n) { for (int i = 0; i < n; ++i) __builtin_amdgcn_s_sleep(1); }
template <int N> __device__ __forceinline__ void sleep_c() { if constexpr (N > 0) __builtin_amdgcn_s_sleep(N); }

constexpr int SC1 = 16;
typedef __amdgpu_buffer_rsrc_t rsrc_t;
__device__ __forceinline__ v4u ld16(rsrc_t rs, unsigned off) { return __builtin_amdgcn_raw_buffer_load_b128(rs, (int)off, 0, SC1); }
__device__ __forceinline__ v2u ld8(rsrc_t rs, unsigned off) { return __builtin_amdgcn_raw_buffer_load_b64(rs, (int)off, 0, SC1); }
__device__ __forceinline__ void st16(rsrc_t rs, unsigned off, v4u v) { __builtin_amdgcn_raw_buffer_store_b128(v, rs, (int)off, 0, SC1); }
__device__ __forceinline__ void st8(rsrc_t rs, unsigned off, v2u v) { __builtin_amdgcn_raw_buffer_store_b64(v, rs, (int)off, 0, SC1); }
__device__ __forceinline__ void st4(rsrc_t rs, unsigned off, unsigned v) { __builtin_amdgcn_raw_buffer_store_b32(v, rs, (int)off, 0, SC1); }
struct Tag { unsigned lo, hi, f0, f1, f2, f3; };
__device__ __forceinline__ Tag make_tag(unsigned lo, unsigned hi) {
  const u64 m = (((u64)hi << 32) | lo) * 0x9E3779B97F4A7C15ull;
  return Tag{lo, hi, (unsigned)m & 0xfffu, (unsigned)(m >> 12) & 0xfffu, (unsigned)(m >> 24) & 0xfffu, (unsigned)(m >> 36) & 0xfffu};
}
__device__ __forceinline__ v4u pack4(f32x4 v, const Tag& t) {
  return v4u{((asu(v[0]) + 0x800u) & 0xfffff000u) | t.f0, ((asu(v[1]) + 0x800u) & 0xfffff000u) | t.f1, ((asu(v[2]) + 0x800u) & 0xfffff000u) | t.f2, ((asu(v[3]) + 0x800u) & 0xfffff000u) | t.f3};
}
__device__ __forceinline__ bool ok4(v4u v, const Tag& t) { return (((v.x ^ t.f0) | (v.y ^ t.f1) | (v.z ^ t.f2) | (v.w ^ t.f3)) & 0xfffu) == 0u; }
__device__ __forceinline__ f32x4 unpack4(v4u v) { return f32x4{asf(v.x & 0xfffff000u), asf(v.y & 0xfffff000u), asf(v.z & 0xfffff000u), asf(v.w & 0xfffff000u)}; }
__device__ __forceinline__ bool okt(v4u v, const Tag& t) { return v.z == t.lo && v.w == t.hi; }
#define POLL_GUARD(sp) if (++(sp) > SPIN_MAX) break; __builtin_amdgcn_s_sleep(1)

#define STAMP(id) do { } while (0)
#define STAMP_DECL do { } while (0)
#define STAMP_FLUSH(lo, hi) do { } while (0)

__device__ __forceinline__ LAS char* opaque(LAS char* q) { unsigned u = (unsigned)(size_t)q; asm volatile("" : "+v"(u)); return (LAS char*)(size_t)u; }
__device__ __forceinline__ f32x4 gemv32_wave(LAS char* hw, LAS char* act, int lane) {
  const int g = lane >> 4, c = lane & 15;
  LAS char* wa = opaque(hw + (8 * g + (c >> 2)) * 64 + (c & 3) * 8);
  LAS char* aa = opaque(act + (c & 3) * 1024 + g * 16);
  f32x4 acc0 = {0.f, 0.f, 0.f, 0.f}, acc1 = {0.f, 0.f, 0.f, 0.f};
#pragma unroll
  for (int bt = 0; bt < 4; ++bt) {
    v2u a0[4], a1[4], a2[4], a3[4]; v4u bb[4];
#pragma unroll
    for (int k = 0; k < 4; ++k) {
      const int ks = bt * 4 + k;
      a0[k] = tr_read(wa + ks * 2048); a1[k] = tr_read(wa + ks * 2048 + 256); a2[k] = tr_read(wa + ks * 2048 + 32); a3[k] = tr_read(wa + ks * 2048 + 32 + 256);
      bb[k] = *(LAS v4u*)(aa + ks * 64);
    }
    __builtin_amdgcn_sched_barrier(0);
#pragma unroll
    for (int k = 0; k < 4; ++k) {
      acc0 = mfma32(v4u{a0[k].x, a0[k].y, a1[k].x, a1[k].y}, bb[k], acc0);
      acc1 = mfma32(v4u{a2[k].x, a2[k].y, a3[k].x, a3[k].y}, bb[k], acc1);
    }
    __builtin_amdgcn_sched_barrier(0);
  }
  return (c & 4) ? acc1 : acc0;
}

__device__ __forceinline__ float silu(float x) {
  const float t = x * -1.4426950408889634f;
  const float r = __builtin_fmaf(x, -1.4426950408889634f, -t) + x * -1.925963033500011e-08f;
  float e = __builtin_amdgcn_exp2f(t);
  e = __builtin_fmaf(e, r * 0.6931471805599453f, e);
  const float d = 1.f + e, rc = __builtin_amdgcn_rcpf(d), q = x * rc;
  const float s = __builtin_fmaf(__builtin_fmaf(-q, d, x), rc, q);
  return e < 3.0e38f ? s : -0.f;
}
__device__ __forceinline__ void ln_stats(const float (&x)[8], float& mb, float& rden) {
  float s = ((x[0] + x[1]) + (x[2] + x[3])) + ((x[4] + x[5]) + (x[6] + x[7]));
  s = wave_sum(s);
  const float mean = s * (1.f / DM);
  float v = 0.f;
#pragma unroll
  for (int j = 0; j < 8; ++j) { const float d = x[j] - mean; v = __builtin_fmaf(d, d, v); }
  v = wave_sum(v);
  mb = rbf(mean);
  rden = __builtin_amdgcn_rcpf(rbf(__builtin_sqrtf(rbf(rbf(v * (1.f / DM)) + 1e-5f))));
}
__device__ __forceinline__ void rbf2(float& a, float& b) { const unsigned u = pk2(a, b); a = bf_lo(u); b = bf_hi(u); }
__device__ __forceinline__ unsigned modulate2(float x0, float x1, float mb, float rden, float c0, float c1, float s0, float s1) {
  x0 -= mb; x1 -= mb; rbf2(x0, x1);
  x0 *= rden; x1 *= rden; rbf2(x0, x1);
  c0 += 1.f; c1 += 1.f; rbf2(c0, c1);
  x0 *= c0; x1 *= c1; rbf2(x0, x1);
  return pk2(x0 + s0, x1 + s1);
}
__device__ __forceinline__ void unpack8(v4u u, float (&f)[8]) {
  f[0] = bf_lo(u.x); f[1] = bf_hi(u.x); f[2] = bf_lo(u.y); f[3] = bf_hi(u.y); f[4] = bf_lo(u.z); f[5] = bf_hi(u.z); f[6] = bf_lo(u.w); f[7] = bf_hi(u.w);
}
__global__ void __launch_bounds__(NTHR, 1) fused_block(Params p) {
  __shared__ __attribute__((aligned(16))) char smem_[163840];
  LAS char* L = (LAS char*)smem_;
  const int tid = threadIdx.x, lane = tid & 63, w = __builtin_amdgcn_readfirstlane(tid >> 6);
  const int blk = blockIdx.x, xl = blk & 7, bi = blk >> 3;
  const int g = lane >> 4, c = lane & 15;
  const unsigned lds0 = (unsigned)(size_t)L;
  const rsrc_t rs = __builtin_amdgcn_make_buffer_rsrc((void*)p.ws, 0, (int)WS_END, 0x00020000);
  const int chunk = xl * 32 + bi;
  const int rb = bi >> 3, cgr = 8 * xl + (bi & 7);
  const bool ffn = bi < 28;
  const unsigned myb = WS_BLK + blk * BLK_STRIDE;
  volatile LAS unsigned* qflag = (volatile LAS unsigned*)(L + L_FLAG);
  constexpr float SCALE = 0.08838834764831845f, LOG2E = 1.4426950408889634f;
  STAMP_DECL;

  if (w == 8) {
    STAMP(0);
    __builtin_amdgcn_s_setprio(3);
    const bool qrole = bi < 16, m1 = !qrole && (bi < 20 || bi >= 28);
    const int hcol = qrole ? 512 * xl + 32 * bi : m1 ? 128 * xl + 32 * (bi & 3) : 1024 + 256 * xl + 32 * (bi - 20);
    if (MK_ABL & 2) {
      if (!(MK_ABL & 8)) {
        const unsigned rstride = qrole ? 8192u : 6144u;
        const char* wb = (qrole ? (const char*)p.wq : (const char*)p.wmod) + hcol * 2;
        const unsigned vo = (lane >> 2) * rstride + (lane & 3) * 16;
        static_for<0, 32>([&](auto t) { dma16<MK_HNT>(wb + t * 16 * rstride, vo, lds0 + L_HW2 + t * 1024); });
      }
      if (lane == 0) { qflag[0] = 1u; qflag[1] = (MK_ABL & 8) ? 1u : 0u; }
      bar(); bar();
      wait_vm<0>(); qflag[1] = 1u;
      bar(); STAMP(8); STAMP_FLUSH(0, 12); return;
    }
    v2u wv;
    asm volatile("buffer_load_dwordx2 %0, %1, %2, 0 offen sc1" : "=v"(wv) : "v"(WS_W), "s"(rs) : "memory");
    {
      const unsigned rstride = qrole ? 8192u : 6144u;
      const char* wb = (qrole ? (const char*)p.wq : (const char*)p.wmod) + hcol * 2;
      const unsigned vo = (lane >> 2) * rstride + (lane & 3) * 16;
      static_for<0, 32>([&](auto t) { dma16<MK_HNT>(wb + t * 16 * rstride, vo, lds0 + L_HW2 + t * 1024); });
    }
    if (lane == 0) { qflag[0] = 0u; qflag[1] = 0u; }
    bar();
    if (MK_M1HOLD && m1) wait_vm<0>();
    bar();
    wait_vm<0>();
    asm volatile("" : "+v"(wv));
    const u64 tag64 = (((u64)wv.y << 32) | wv.x) + 1ull;
    const Tag tg = make_tag((unsigned)tag64, (unsigned)(tag64 >> 32));
    if (lane == 0) *(LAS v2u*)(L + L_TAG) = v2u{tg.lo, tg.hi};
    STAMP(22);
    if (!qrole) {
      const f32x4 mo = gemv32_wave(L + L_HW2, L + L_ACT, lane);
      unsigned d0 = pk2(mo[0], mo[1]), d1 = pk2(mo[2], mo[3]);
      const int col = hcol + 16 * ((c >> 2) & 1) + 4 * g;
      if ((hcol & ~511) == 512 || (hcol & ~511) == 2048) { d0 = pk2(bf_lo(d0) + 1.f, bf_hi(d0) + 1.f); d1 = pk2(bf_lo(d1) + 1.f, bf_hi(d1) + 1.f); }
      if (c < 8) st16(rs, WS_MT + (c & 3) * 12288 + (col >> 2) * 16, v4u{d0, d1, tg.lo, tg.hi});
      wait_lgkm0();
      qflag[1] = 1u;
    } else {
      sleep_c<MK_DM>();
      v4u sa[4][2], ca[4][2];
      for (unsigned sp = 0;;) {
        bool ok = true;
#pragma unroll
        for (int b = 0; b < 4; ++b)
#pragma unroll
          for (int j = 0; j < 2; ++j) { sa[b][j] = ld16(rs, WS_MT + b * 12288 + lane * 32 + j * 16); ca[b][j] = ld16(rs, WS_MT + b * 12288 + 2048 + lane * 32 + j * 16); }
#pragma unroll
        for (int b = 0; b < 4; ++b)
#pragma unroll
          for (int j = 0; j < 2; ++j) ok = ok && okt(sa[b][j], tg) && okt(ca[b][j], tg);
        if (__all(ok)) break;
        if (++sp > SPIN_MAX) break;
        sleep_c<MK_DP>();
      }
      STAMP(2);
#pragma unroll
      for (int b = 0; b < 4; ++b) {
        float n[8], a[8], sh[8];
        unpack8(*(LAS v4u*)(L + L_XN + b * 1024 + lane * 16), n);
        unpack8(v4u{ca[b][0].x, ca[b][0].y, ca[b][1].x, ca[b][1].y}, a); unpack8(v4u{sa[b][0].x, sa[b][0].y, sa[b][1].x, sa[b][1].y}, sh);
        unsigned h[4];
#pragma unroll
        for (int j = 0; j < 4; ++j) {
          float x0 = n[2 * j] * a[2 * j], x1 = n[2 * j + 1] * a[2 * j + 1]; rbf2(x0, x1);
          h[j] = pk2(x0 + sh[2 * j], x1 + sh[2 * j + 1]);
        }
        *(LAS v4u*)(L + L_ACT + b * 1024 + lane * 16) = v4u{h[0], h[1], h[2], h[3]};
      }
      STAMP(4);
      const f32x4 qo = gemv32_wave(L + L_HW2, L + L_ACT, lane);
      if (c < 8) st16(rs, WS_QT + (c & 3) * 16384 + ((hcol + 16 * (c >> 2) + 4 * g) >> 2) * 16, v4u{pk2(qo[0], qo[1]), pk2(qo[2], qo[3]), tg.lo, tg.hi});
      wait_lgkm0();
      qflag[1] = 1u;
    }
    STAMP(5);
    if (qrole) sleep_c<MK_DQ>(); else sleep_c<MK_DQ2>();
    {
      v4u q0, q1;
      const unsigned qoff = WS_QT + (chunk >> 6) * 16384 + ((chunk >> 3) & 7) * 2048 + lane * 32;
      for (unsigned sp = 0;;) {
        q0 = ld16(rs, qoff); q1 = ld16(rs, qoff + 16);
        if (__all(okt(q0, tg) && okt(q1, tg))) break;
        if (++sp > SPIN_MAX) break;
        sleep_c<MK_DP>();
      }
      *(LAS v4u*)(L + L_QS + lane * 16) = v4u{q0.x, q0.y, q1.x, q1.y};
      wait_lgkm0();
      *qflag = 1u;
    }
    STAMP(6);
    float gav[4], gbv[4], xres[4];
    {
      v4u c1, s2, ga, gb;
      const unsigned go = WS_MT + rb * 12288 + (2 * cgr + (lane & 1)) * 16;
      const unsigned co = WS_MT + g * 12288 + (16 * xl + c) * 16;
      const v2u xr = *(const GAS v2u*)(p.x + rb * DM + 8 * cgr + 4 * (lane & 1));
      for (unsigned sp = 0;;) {
        c1 = ld16(rs, co + 8192); s2 = ld16(rs, co + 6144); ga = ld16(rs, go + 4096); gb = ld16(rs, go + 10240);
        if (__all(okt(c1, tg) && okt(s2, tg) && okt(ga, tg) && okt(gb, tg))) break;
        if (++sp > SPIN_MAX) break;
        sleep_c<MK_DP>();
      }
      *(LAS f32x4*)(L + L_C1 + g * 256 + c * 16) = f32x4{bf_lo(c1.x), bf_hi(c1.x), bf_lo(c1.y), bf_hi(c1.y)};
      *(LAS f32x4*)(L + L_S2 + g * 256 + c * 16) = f32x4{bf_lo(s2.x), bf_hi(s2.x), bf_lo(s2.y), bf_hi(s2.y)};
      gav[0] = bf_lo(ga.x); gav[1] = bf_hi(ga.x); gav[2] = bf_lo(ga.y); gav[3] = bf_hi(ga.y);
      gbv[0] = bf_lo(gb.x); gbv[1] = bf_hi(gb.x); gbv[2] = bf_lo(gb.y); gbv[3] = bf_hi(gb.y);
      xres[0] = bf_lo(xr.x); xres[1] = bf_hi(xr.x); xres[2] = bf_lo(xr.y); xres[3] = bf_hi(xr.y);
    }
    STAMP(7);
    bar();
    STAMP(8);
    if (MK_ABL & 1) { STAMP_FLUSH(0, 12); STAMP_FLUSH(22, 24); return; }
    bar();
    const int pr = lane >> 1;
    const unsigned tcol = (64 * (xl & 1) + 8 * (bi & 7) + 4 * (lane & 1)) * 4 + rb * 512;
    float x1[4];
    sleep_c<MK_D2>();
    {
      const unsigned a0 = WS_BLK + (8 * (4 * (pr >> 3) + (xl >> 1)) + (pr & 7)) * BLK_STRIDE + BLK_AP + tcol, a1 = a0 + 128 * BLK_STRIDE;
      v4u va, vb;
      for (unsigned sp = 0;;) { va = ld16(rs, a0); vb = ld16(rs, a1); if (__all(ok4(va, tg) && ok4(vb, tg))) break; POLL_GUARD(sp); }
      const f32x4 s = unpack4(va) + unpack4(vb);
#pragma unroll
      for (int j = 0; j < 4; ++j) {
        float t = s[j];
        t += dpp<XOR2>(t); t += dpp<ROR4>(t); t += dpp<ROR8>(t); t = x16_sum(t);
        x1[j] = rbf(xres[j] + rbf(gav[j] * rbf(t)));
      }
      if (lane < 2) st16(rs, WS_XT + rb * 2048 + (2 * cgr + lane) * 16, v4u{pk2(x1[0], x1[1]), pk2(x1[2], x1[3]), tg.lo, tg.hi});
    }
    STAMP(10);
    bar();
    bar();
    if (ffn) {
      sleep_c<MK_D4>();
      const int fq = 7 * xl + (bi >> 2);
      const unsigned po = WS_BLK + 8 * (fq >> 1) * BLK_STRIDE + BLK_PP + (lane >> 5) * 1024 + ((lane >> 3) & 3) * 256 + ((fq & 1) * 32 + (lane & 7) * 4) * 4;
      v4u v[8];
      for (unsigned sp = 0;;) {
        bool ok = true;
#pragma unroll
        for (int k = 0; k < 8; ++k) v[k] = ld16(rs, po + k * BLK_STRIDE);
#pragma unroll
        for (int k = 0; k < 8; ++k) ok = ok && ok4(v[k], tg);
        if (__all(ok)) break;
        POLL_GUARD(sp);
      }
      f32x4 s = unpack4(v[0]);
#pragma unroll
      for (int k = 1; k < 8; ++k) s += unpack4(v[k]);
      float gl[4];
#pragma unroll
      for (int j = 0; j < 4; ++j) {
        const float t = rbf(s[j]);
        auto sw = __builtin_amdgcn_permlane32_swap(asu(t), asu(t), false, false);
        const float up = asf(sw[1]);
        const float z = 0.7978845608028654f * (t + 0.044715f * t * t * t);
        const float th = 1.f - 2.f * __builtin_amdgcn_rcpf(1.f + __builtin_amdgcn_exp2f(z * (2.f * LOG2E)));
        gl[j] = rbf(0.5f * t * (1.f + th)) * up;
      }
      if (lane < 32) *(LAS v2u*)(L + L_GB + (lane >> 3) * 64 + (lane & 7) * 8) = v2u{pk2(gl[0], gl[1]), pk2(gl[2], gl[3])};
    }
    STAMP(11);
    bar();
    sleep_c<MK_D5>();
    {
      const unsigned a0 = WS_BLK + (8 * (4 * (pr >> 3) + (xl >> 1)) + (pr & 7)) * BLK_STRIDE + BLK_YP + tcol;
      const bool two = pr < 24;
      const unsigned a1 = two ? a0 + 128 * BLK_STRIDE : a0;
      v4u va, vb;
      for (unsigned sp = 0;;) { va = ld16(rs, a0); vb = ld16(rs, a1); if (__all(ok4(va, tg) && ok4(vb, tg))) break; POLL_GUARD(sp); }
      f32x4 s = unpack4(va);
      if (two) s += unpack4(vb);
      float o[4];
#pragma unroll
      for (int j = 0; j < 4; ++j) {
        float t = s[j];
        t += dpp<XOR2>(t); t += dpp<ROR4>(t); t += dpp<ROR8>(t); t = x16_sum(t);
        o[j] = x1[j] + rbf(gbv[j] * rbf(t));
      }
      if (lane < 2) *(GAS v2u*)(p.out + rb * DM + 8 * cgr + 4 * lane) = v2u{pk2(o[0], o[1]), pk2(o[2], o[3])};
      if (blk == 0 && lane == 0) st8(rs, WS_W, v2u{tg.lo, tg.hi});
    }
    STAMP(9);
    STAMP_FLUSH(0, 12);
    STAMP_FLUSH(22, 24);
    return;
  }

  const char* kb = (const char*)p.kc + (size_t)chunk * 262144 + w * 32768;
  const char* vb_ = (const char*)p.vc + (size_t)chunk * 262144 + w * 32768;
  const unsigned kreg = lds0 + L_K + w * 16384;
  unsigned ko[4];
#pragma unroll
  for (int j = 0; j < 4; ++j) ko[j] = j * 1024 + g * 256 + ((c ^ (4 * j + g)) * 16);
  const unsigned vo = g * 1024 + c * 16;
  constexpr int W = MK_WIN;
  v4u V[20];
  v4u cv;
  gload(cv, (bi < 16 ? p.x : p.c) + (w & 3) * DM, lane * 16);
  bar();
  auto kfill = [&](auto half, auto lo, auto hi, auto first) {
    static_for<lo, hi>([&](auto ii) { constexpr int i = ii; if constexpr (i >= first) wait_vm<W - 1>(); dma16<1>(kb + half * 16384 + (i >> 2) * 4096, ko[i & 3], kreg + i * 1024); });
  };
  using I0 = std::integral_constant<int, 0>; using I1 = std::integral_constant<int, 1>; using IW = std::integral_constant<int, W>; using I16 = std::integral_constant<int, 16>;
  if (w != 7) kfill(I0{}, I0{}, IW{}, IW{});
  if (w == 7) wait_vm<0>(); else wait_vm<W>();
  pin(cv);
  if (w < 4) {
    float f[8];
    unpack8(cv, f);
    if (bi < 16) {
      float mb, rden;
      ln_stats(f, mb, rden);
      unsigned n[4];
#pragma unroll
      for (int j = 0; j < 4; ++j) { float x0 = f[2 * j] - mb, x1 = f[2 * j + 1] - mb; rbf2(x0, x1); n[j] = pk2(x0 * rden, x1 * rden); }
      *(LAS v4u*)(L + L_XN + w * 1024 + lane * 16) = v4u{n[0], n[1], n[2], n[3]};
    } else {
#pragma unroll
      for (int j = 0; j < 8; ++j) f[j] = silu(f[j]);
      *(LAS v4u*)(L + L_ACT + w * 1024 + lane * 16) = v4u{pk2(f[0], f[1]), pk2(f[2], f[3]), pk2(f[4], f[5]), pk2(f[6], f[7])};
    }
  }
  bar();
  if (w != 7) kfill(I0{}, IW{}, I16{}, I0{});
  static_for<0, 20>([&](auto jj) { constexpr int j = jj; wait_vm<W - 1>(); gload_nt<(j & 3) * 256>(V[j], vb_ + (j >> 2) * 4096, vo); });
  if (w == 7) {
    for (unsigned sp = 0; qflag[1] == 0u;) { if (++sp > (SPIN_MAX << 4)) break; __builtin_amdgcn_s_sleep(1); }
    asm volatile("" ::: "memory");
    kfill(I0{}, I0{}, I16{}, std::integral_constant<int, MK_W7>{});
  }
  if (w == 0) STAMP(12);
  for (unsigned sp = 0; *qflag == 0u;) { if (++sp > (SPIN_MAX << 4)) break; __builtin_amdgcn_s_sleep(1); }
  if (w == 0) STAMP(13);
  asm volatile("" ::: "memory");
  v4u qf[4];
#pragma unroll
  for (int ks = 0; ks < 4; ++ks) qf[ks] = *(LAS v4u*)(L + L_QS + (c >> 2) * 256 + ks * 64 + g * 16);
  LAS char* kread[4];
#pragma unroll
  for (int ks = 0; ks < 4; ++ks) kread[ks] = L + L_K + w * 16384 + c * 256 + (((4 * ks) ^ (g ^ c)) * 16);
  auto qk = [&](f32x4 (&s)[4]) {
#pragma unroll
    for (int t = 0; t < 4; ++t) {
      f32x4 a = {0.f, 0.f, 0.f, 0.f};
#pragma unroll
      for (int ks = 0; ks < 4; ++ks) a = mfma32(*(LAS v4u*)(kread[ks] + t * 4096), qf[ks], a);
      s[t] = a;
    }
  };
  const int jp = c & 3;
  const unsigned selE = (jp & 1) ? 0x01000c0cu : 0x0c0c0100u, selO = (jp & 1) ? 0x03020c0cu : 0x0c0c0302u, selZ = 0x0c0c0c0cu;
  const unsigned s00 = jp < 2 ? selE : selZ, s01 = jp < 2 ? selZ : selE, s10 = jp < 2 ? selO : selZ, s11 = jp < 2 ? selZ : selO;
  float mrun, lrun = 0.f;
  f32x4 acc1 = {0.f, 0.f, 0.f, 0.f}, acc2 = {0.f, 0.f, 0.f, 0.f};
  unsigned pk[8];
  auto scores = [&](f32x4 (&s)[4]) -> float {
    float mx = -INFINITY;
#pragma unroll
    for (int t = 0; t < 4; ++t) {
      const unsigned u0 = pk2(s[t][0], s[t][1]), u1 = pk2(s[t][2], s[t][3]);
      const unsigned v0 = pk2(bf_lo(u0) * SCALE, bf_hi(u0) * SCALE), v1 = pk2(bf_lo(u1) * SCALE, bf_hi(u1) * SCALE);
      s[t] = f32x4{bf_lo(v0), bf_hi(v0), bf_lo(v1), bf_hi(v1)};
      mx = fmaxf(fmaxf(mx, fmaxf(s[t][0], s[t][1])), fmaxf(s[t][2], s[t][3]));
    }
    return x16_max(mx);
  };
  auto probs = [&](f32x4 (&s)[4], float m) {
#pragma unroll
    for (int t = 0; t < 4; ++t) {
      float e[4];
#pragma unroll
      for (int r = 0; r < 4; ++r) { e[r] = __builtin_amdgcn_exp2f((s[t][r] - m) * LOG2E); lrun += e[r]; }
      pk[2 * t] = pk2(e[0], e[1]); pk[2 * t + 1] = pk2(e[2], e[3]);
    }
  };
  auto pv = [&](unsigned pr, int odd, const v4u& v) {
    const v2u a = {__builtin_amdgcn_perm(pr, pr, odd ? s10 : s00), __builtin_amdgcn_perm(pr, pr, odd ? s11 : s01)};
    acc1 = mfma16(a, v2u{v.x, v.y}, acc1);
    acc2 = mfma16(a, v2u{v.z, v.w}, acc2);
  };

  f32x4 s[4];
  if (w == 7) wait_vm<0>(); else wait_vm<20>();
  qk(s);
  wait_lgkm0();
  __builtin_amdgcn_sched_barrier(0);
  kfill(I1{}, I0{}, IW{}, IW{});
  __builtin_amdgcn_sched_barrier(0);
  mrun = scores(s);
  probs(s, mrun);
  wait_vm<4 + W>();
  static_for<0, 16>([&](auto ii) { constexpr int i = ii; pin(V[i]); });
  __builtin_amdgcn_sched_barrier(0);
  static_for<0, 16>([&](auto ii) { constexpr int i = ii; pv(pk[i >> 1], i & 1, V[i]); });
  __builtin_amdgcn_sched_barrier(0);
  kfill(I1{}, IW{}, I16{}, I0{});
  v4u V2[12];
  static_for<0, W>([&](auto ii) { constexpr int i = ii; wait_vm<W - 1>(); gload_nt<(i & 3) * 256>(V2[i], vb_ + 16384 + 4096 + (i >> 2) * 4096, vo); });
  if (w == 0) STAMP(14);
  wait_vm<W>();
  qk(s);
  wait_lgkm0();
  __builtin_amdgcn_sched_barrier(0);
  static_for<W, W + 2>([&](auto ii) { constexpr int i = ii; gload_nt<(i & 3) * 256>(V2[i], vb_ + 16384 + 4096 + (i >> 2) * 4096, vo); });
  __builtin_amdgcn_sched_barrier(0);
  {
    const float m2 = scores(s);
    const float mn = fmaxf(mrun, m2);
    const float al = __builtin_amdgcn_exp2f((mrun - mn) * LOG2E);
    lrun *= al;
    const float a0 = asf(__builtin_amdgcn_readlane(asu(al), 0)), a1 = asf(__builtin_amdgcn_readlane(asu(al), 4));
    const float a2 = asf(__builtin_amdgcn_readlane(asu(al), 8)), a3 = asf(__builtin_amdgcn_readlane(asu(al), 12));
    const float af = g == 0 ? a0 : g == 1 ? a1 : g == 2 ? a2 : a3;
    acc1 *= af; acc2 *= af;
    mrun = mn;
    probs(s, mrun);
  }
  static_for<0, 4>([&](auto ii) { constexpr int i = ii; pin(V[16 + i]); });
  __builtin_amdgcn_sched_barrier(0);
  static_for<0, 4>([&](auto ii) { constexpr int i = ii; pv(pk[i >> 1], i & 1, V[16 + i]); });
  __builtin_amdgcn_sched_barrier(0);
  static_for<W + 2, 12>([&](auto ii) { constexpr int i = ii; wait_vm<W - 1>(); gload_nt<(i & 3) * 256>(V2[i], vb_ + 16384 + 4096 + (i >> 2) * 4096, vo); });
  constexpr int NTW = (MK_ABL & 4) ? 0 : 5;
  if (!(MK_ABL & 4)) {
    const char* wo_t = (const char*)p.wo + (size_t)(xl * 512 + (bi >> 2) * 64 + 8 * w) * 1024 + (bi & 3) * 256;
    dma16<1>(wo_t, vo, kreg + R_WO); dma16<1>(wo_t + 4096, vo, kreg + R_WO + 1024);
    const int fb = ffn ? bi : 0;
    const unsigned gvo = (lane >> 3) * 3584 + (lane & 7) * 16;
    dma16<1>((const char*)p.wg + (size_t)(64 * xl + 8 * w) * 3584 + fb * 128, gvo, kreg + R_G);
    dma16<1>((const char*)p.wu + (size_t)(64 * xl + 8 * w) * 3584 + fb * 128, gvo, kreg + R_U);
    dma16<1>((const char*)p.wd + (size_t)(32 * (7 * xl + (fb >> 2)) + 4 * w) * 1024 + (fb & 3) * 256, vo, kreg + R_DN);
  }
  __builtin_amdgcn_sched_barrier(0);
  static_for<0, 12>([&](auto ii) {
    constexpr int i = ii;
    wait_vm_on<11 - i + NTW>(V2[i]);
    __builtin_amdgcn_sched_barrier(0);
    pv(pk[2 + (i >> 1)], i & 1, V2[i]);
    __builtin_amdgcn_sched_barrier(0);
  });
  if (w == 0) STAMP(15);
  {
    const float lw = x16_sum(lrun);
    LAS char* om = L + L_K + w * 16384 + R_OM + g * 256 + c * 16;
    *(LAS f32x4*)(om) = acc1;
    *(LAS f32x4*)(om + 1024 + 128) = acc2;
    if (g == 0 && jp == 0) { ((LAS float*)(L + L_MW))[w * 4 + (c >> 2)] = mrun; ((LAS float*)(L + L_LW))[w * 4 + (c >> 2)] = lw; }
  }
  bar();
  if (MK_ABL & 1) { if (w == 0) STAMP_FLUSH(12, 21); if (acc1[0] == 1.2345f) p.out[0] = 1; return; }
  const v2u tw = *(LAS v2u*)(L + L_TAG);
  const Tag tg = make_tag(tw.x, tw.y);
  if (w >= 4 && w < 6) {
    const int t = tid - 256, h = t >> 5, cc = (t >> 1) & 15, pl = t & 1;
    float mw[8], M = -INFINITY;
#pragma unroll
    for (int i = 0; i < 8; ++i) { mw[i] = ((LAS float*)(L + L_MW))[i * 4 + h]; M = fmaxf(M, mw[i]); }
    f32x4 o = {0.f, 0.f, 0.f, 0.f}; float l = 0.f;
#pragma unroll
    for (int i = 0; i < 8; ++i) {
      const float f = __builtin_amdgcn_exp2f((mw[i] - M) * LOG2E);
      o += f * *(LAS f32x4*)(L + L_K + i * 16384 + R_OM + pl * (1024 + 128) + h * 256 + cc * 16);
      l += f * ((LAS float*)(L + L_LW))[i * 4 + h];
    }
    st16(rs, myb + BLK_OP + (h * 128 + 8 * cc + 4 * pl) * 4, pack4(o, tg));
    if ((t & 31) == 0) st16(rs, myb + BLK_ML + h * 16, v4u{asu(M), asu(l), tg.lo, tg.hi});
  }
  if (w < 4) {
    sleep_c<MK_D1>();
    const int ca_ = 64 * w + 8 * xl + g, cb_ = ca_ + 4, j = bi >> 2;
    const unsigned ba = WS_BLK + (8 * (ca_ & 31) + (ca_ >> 5)) * BLK_STRIDE, bb = WS_BLK + (8 * (cb_ & 31) + (cb_ >> 5)) * BLK_STRIDE;
    const unsigned oo = BLK_OP + (j * 64 + 4 * c) * 4, mo_ = BLK_ML + (j >> 1) * 16;
    v4u va, vb, la, lb;
    for (unsigned sp = 0;;) {
      va = ld16(rs, ba + oo); vb = ld16(rs, bb + oo); la = ld16(rs, ba + mo_); lb = ld16(rs, bb + mo_);
      if (__all(ok4(va, tg) && ok4(vb, tg) && okt(la, tg) && okt(lb, tg))) break;
      POLL_GUARD(sp);
    }
    const float m_a = asf(la.x), m_b = asf(lb.x);
    const float M = x16_max(fmaxf(m_a, m_b));
    const float wa = __builtin_amdgcn_exp2f((m_a - M) * LOG2E), wb = __builtin_amdgcn_exp2f((m_b - M) * LOG2E);
    const float inv = __builtin_amdgcn_rcpf(x16_sum(wa * asf(la.y) + wb * asf(lb.y)));
    const f32x4 t = wa * unpack4(va) + wb * unpack4(vb);
    const float o0 = x16_sum(t[0]) * inv, o1 = x16_sum(t[1]) * inv, o2 = x16_sum(t[2]) * inv, o3 = x16_sum(t[3]) * inv;
    if (g == 0) *(LAS v2u*)(L + L_OB + w * 128 + c * 8) = v2u{pk2(o0, o1), pk2(o2, o3)};
    if (w == 0) STAMP(16);
  }
  if (w == 4 || w == 5) wait_vm<2>(); else wait_vm<0>();
  bar();
  const int q4 = c >> 2, p4 = c & 3;
  v4u wa2[2], ga2[2], da;
  {
    LAS char* wr = opaque(L + L_K + g * 16384 + R_WO + q4 * 256 + w * 32 + p4 * 8);
#pragma unroll
    for (int ks = 0; ks < 2; ++ks) { const v2u a0 = tr_read(wr + ks * 65536), a1 = tr_read(wr + ks * 65536 + 1024); wa2[ks] = v4u{a0.x, a0.y, a1.x, a1.y}; }
  }
  {
    const v4u b0 = *(LAS v4u*)(L + L_OB + (c & 3) * 128 + g * 16), b1 = *(LAS v4u*)(L + L_OB + (c & 3) * 128 + 64 + g * 16);
    f32x4 d = mfma32(wa2[0], b0, f32x4{0.f, 0.f, 0.f, 0.f});
    d = mfma32(wa2[1], b1, d);
    if (c < 4) st16(rs, myb + BLK_AP + c * 512 + (16 * w + 4 * g) * 4, pack4(d, tg));
  }
  if (w == 0) STAMP(17);
  {
    LAS char* gr = opaque(L + L_K + g * 16384 + (w < 4 ? R_G : R_U) + q4 * 128 + (w & 3) * 32 + p4 * 8);
#pragma unroll
    for (int ks = 0; ks < 2; ++ks) { const v2u a0 = tr_read(gr + ks * 65536), a1 = tr_read(gr + ks * 65536 + 512); ga2[ks] = v4u{a0.x, a0.y, a1.x, a1.y}; }
    LAS char* dr = L + L_K + R_DN + q4 * 256 + w * 32 + p4 * 8;
    const v2u a0 = tr_read(dr + (2 * g) * 16384), a1 = tr_read(dr + (2 * g + 1) * 16384);
    da = v4u{a0.x, a0.y, a1.x, a1.y};
  }
  bar();
  if (w < 4 && ffn) {
    sleep_c<MK_D3>();
    v4u x0, x1, xs;
    const unsigned xo = WS_XT + w * 2048;
    for (unsigned sp = 0;;) {
      x0 = ld16(rs, xo + lane * 32); x1 = ld16(rs, xo + lane * 32 + 16); xs = ld16(rs, xo + (16 * xl + (lane >> 2)) * 16);
      if (__all(okt(x0, tg) && okt(x1, tg) && okt(xs, tg))) break;
      POLL_GUARD(sp);
    }
    float xf[8], mb, rden;
    unpack8(v4u{x0.x, x0.y, x1.x, x1.y}, xf);
    ln_stats(xf, mb, rden);
    const unsigned xw = (lane & 2) ? xs.y : xs.x;
    float v = ((lane & 1) ? bf_hi(xw) : bf_lo(xw)) - mb;
    v = rbf(rbf(v) * rden);
    v = rbf(v * ((LAS float*)(L + L_C1))[w * 64 + lane]) + ((LAS float*)(L + L_S2))[w * 64 + lane];
    *(LAS u16*)(L + L_H2 + w * 128 + lane * 2) = (u16)(pk2(v, v) & 0xffffu);
  }
  if (w == 0) STAMP(18);
  bar();
  if (ffn) {
    const v4u b0 = *(LAS v4u*)(L + L_H2 + (c & 3) * 128 + g * 16), b1 = *(LAS v4u*)(L + L_H2 + (c & 3) * 128 + 64 + g * 16);
    f32x4 d = mfma32(ga2[0], b0, f32x4{0.f, 0.f, 0.f, 0.f});
    d = mfma32(ga2[1], b1, d);
    if (c < 4) st16(rs, myb + BLK_PP + (w >> 2) * 1024 + c * 256 + (16 * (w & 3) + 4 * g) * 4, pack4(d, tg));
  }
  if (w == 0) STAMP(19);
  bar();
  if (ffn) {
    const v4u b0 = *(LAS v4u*)(L + L_GB + (c & 3) * 64 + g * 16);
    const f32x4 d = mfma32(da, b0, f32x4{0.f, 0.f, 0.f, 0.f});
    if (c < 4) st16(rs, myb + BLK_YP + c * 512 + (16 * w + 4 * g) * 4, pack4(d, tg));
  }
  if (w == 0) { STAMP(20); STAMP_FLUSH(12, 21); }
}

}

extern "C" void kernel_launch(void* const* d_in, const int* in_sizes, int n_in, void* d_out, int out_size, void* d_ws, size_t ws_size, hipStream_t stream) {
  mk::Params p;
  p.x = (const mk::u16*)d_in[0]; p.kc = (const mk::u16*)d_in[1]; p.vc = (const mk::u16*)d_in[2];
  p.wq = (const mk::u16*)d_in[3]; p.wo = (const mk::u16*)d_in[4];
  p.wg = (const mk::u16*)d_in[6]; p.wu = (const mk::u16*)d_in[7]; p.wd = (const mk::u16*)d_in[8];
  p.c = (const mk::u16*)d_in[9]; p.wmod = (const mk::u16*)d_in[10];
  p.out = (mk::u16*)d_out; p.ws = (char*)d_ws;
  hipLaunchKernelGGL(mk::fused_block, dim3(mk::NBLK), dim3(mk::NTHR), 0, stream, p);
}
```

```cpp
#include <hip/hip_runtime.h>
#include <hip/hip_bf16.h>
#include <stdint.h>
#include <type_traits>

namespace mk {

typedef unsigned short u16;
typedef unsigned long long u64;
typedef unsigned v4u __attribute__((ext_vector_type(4)));
typedef unsigned v2u __attribute__((ext_vector_type(2)));
typedef float f32x4 __attribute__((ext_vector_type(4)));
typedef float f32x2 __attribute__((ext_vector_type(2)));
typedef short s16x4 __attribute__((ext_vector_type(4)));
typedef __bf16 bf16x8 __attribute__((ext_vector_type(8)));
typedef __bf16 bf16x2 __attribute__((ext_vector_type(2)));
#define LAS __attribute__((address_space(3)))
#define GAS __attribute__((address_space(1)))

#define MK_WIN 3
#define MK_D1 24
#define MK_D2 12
#define MK_D3 12
#define MK_D4 14
#define MK_D5 14
#define MK_DQ2 64
#define MK_W7 8
#define MK_DQ 10
#define MK_DM 32
#define MK_HNT 1
#define MK_WARM 1
#define MK_M1HOLD 1
#define MK_NOQWAIT 0
#define MK_ABL 0
#define MK_KNT 1
#define MK_VNT 1
#define MK_TNT 1
#define MK_DP 8
#define MK_STAMPS 0

constexpr int NB = 4, DM = 512, HQ = 32, HKV = 8, SK = 8192, DH = 128, FF = 1792;
constexpr int NBLK = 256, NTHR = 576;
constexpr unsigned SPIN_MAX = 1u << 17;

constexpr unsigned WS_W = 0;
constexpr unsigned WS_MT = 4096;
constexpr unsigned WS_QT = WS_MT + 49152;
constexpr unsigned WS_XT = WS_QT + 65536;
constexpr unsigned WS_BLK = WS_XT + 8192;
constexpr unsigned BLK_OP = 0;
constexpr unsigned BLK_AP = 2048;
constexpr unsigned BLK_PP = 4096;
constexpr unsigned BLK_YP = 6144;
constexpr unsigned BLK_ML = 8192;
constexpr unsigned BLK_STRIDE = 8320;
constexpr unsigned WS_END = WS_BLK + NBLK * BLK_STRIDE;
constexpr unsigned WS_STAMP = 8u << 20;

constexpr unsigned L_K = 0;
constexpr unsigned L_HW2 = 114688;
constexpr unsigned L_ACT = 131072 + 16384;
constexpr unsigned L_XN = L_ACT + 4096;
constexpr unsigned L_QS = L_XN + 4096;
constexpr unsigned L_C1 = L_QS + 1024;
constexpr unsigned L_S2 = L_C1 + 1024;
constexpr unsigned L_MW = L_S2 + 1024;
constexpr unsigned L_LW = L_MW + 128;
constexpr unsigned L_OB = L_LW + 128;
constexpr unsigned L_H2 = L_OB + 512;
constexpr unsigned L_GB = L_H2 + 512;
constexpr unsigned L_TAG = L_GB + 256;
constexpr unsigned L_FLAG = L_TAG + 16;
constexpr unsigned L_END = L_FLAG + 16;
static_assert(L_END <= 163840, "LDS");
constexpr unsigned R_WO = 0;
constexpr unsigned R_G = 2048;
constexpr unsigned R_U = 3072;
constexpr unsigned R_DN = 4096;
constexpr unsigned R_OM = 8192;

struct Params {
  const u16 *x, *kc, *vc, *wq, *wo, *wg, *wu, *wd, *c, *wmod;
  u16* out;
  char* ws;
};

template <int I, int N, class F> __device__ __forceinline__ void static_for(F&& f) {
  if constexpr (I < N) { f(std::integral_constant<int, I>{}); static_for<I + 1, N>(f); }
}
__device__ __forceinline__ float bf_lo(unsigned u) { return __builtin_bit_cast(float, u << 16); }
__device__ __forceinline__ float bf_hi(unsigned u) { return __builtin_bit_cast(float, u & 0xffff0000u); }
__device__ __forceinline__ unsigned pk2(float a, float b) { f32x2 f = {a, b}; return __builtin_bit_cast(unsigned, __builtin_convertvector(f, bf16x2)); }
__device__ __forceinline__ float rbf(float a) { return bf_lo(pk2(a, a)); }
__device__ __forceinline__ float asf(unsigned u) { return __builtin_bit_cast(float, u); }
__device__ __forceinline__ unsigned asu(float f) { return __builtin_bit_cast(unsigned, f); }
template <int CTRL> __device__ __forceinline__ float dpp(float x) { return asf((unsigned)__builtin_amdgcn_mov_dpp((int)asu(x), CTRL, 0xf, 0xf, true)); }
template <int CTRL> __device__ __forceinline__ unsigned dppu(unsigned x) { return (unsigned)__builtin_amdgcn_mov_dpp((int)x, CTRL, 0xf, 0xf, true); }
constexpr int XOR1 = 0xB1, XOR2 = 0x4E, HMIR = 0x141, ROR4 = 0x124, ROR8 = 0x128;
__device__ __forceinline__ float x16_max(float x) {
  auto s = __builtin_amdgcn_permlane16_swap(asu(x), asu(x), false, false); x = fmaxf(asf(s[0]), asf(s[1]));
  auto t = __builtin_amdgcn_permlane32_swap(asu(x), asu(x), false, false); return fmaxf(asf(t[0]), asf(t[1]));
}
__device__ __forceinline__ float x16_sum(float x) {
  auto s = __builtin_amdgcn_permlane16_swap(asu(x), asu(x), false, false); x = asf(s[0]) + asf(s[1]);
  auto t = __builtin_amdgcn_permlane32_swap(asu(x), asu(x), false, false); return asf(t[0]) + asf(t[1]);
}
__device__ __forceinline__ float row_sum(float x) {
  x += dpp<XOR1>(x); x += dpp<XOR2>(x); x += dpp<ROR4>(x); x += dpp<ROR8>(x); return x;
}
__device__ __forceinline__ float wave_sum(float x) { return x16_sum(row_sum(x)); }
__device__ __forceinline__ f32x4 mfma32(v4u a, v4u b, f32x4 c) {
  return __builtin_amdgcn_mfma_f32_16x16x32_bf16(__builtin_bit_cast(bf16x8, a), __builtin_bit_cast(bf16x8, b), c, 0, 0, 0);
}
__device__ __forceinline__ f32x4 mfma16(v2u a, v2u b, f32x4 c) {
  return __builtin_amdgcn_mfma_f32_16x16x16bf16_1k(__builtin_bit_cast(s16x4, a), __builtin_bit_cast(s16x4, b), c, 0, 0, 0);
}
__device__ __forceinline__ float dot2(unsigned a, unsigned b, float c) {
  return __builtin_amdgcn_fdot2_f32_bf16(__builtin_bit_cast(bf16x2, a), __builtin_bit_cast(bf16x2, b), c, false);
}
__device__ __forceinline__ v2u tr_read(LAS char* p) { return __builtin_bit_cast(v2u, __builtin_amdgcn_ds_read_tr16_b64_v4i16((LAS s16x4*)p)); }
template <int N> __device__ __forceinline__ void wait_vm() { asm volatile("s_waitcnt vmcnt(%0)" ::"i"(N) : "memory"); }
__device__ __forceinline__ void wait_lgkm0() { asm volatile("s_waitcnt lgkmcnt(0)" ::: "memory"); }
__device__ __forceinline__ void bar() { asm volatile("s_waitcnt lgkmcnt(0)\n\ts_barrier" ::: "memory"); }
template <int NT> __device__ __forceinline__ void dma16(const void* base, unsigned voff, unsigned lds) {
  unsigned keep;
  if constexpr (NT)
    asm volatile("s_mov_b32 %0, m0\n\ts_mov_b32 m0, %3\n\ts_nop 0\n\tglobal_load_lds_dwordx4 %1, %2 nt\n\ts_mov_b32 m0, %0" : "=&s"(keep) : "v"(voff), "s"(base), "s"(lds) : "memory");
  else
    asm volatile("s_mov_b32 %0, m0\n\ts_mov_b32 m0, %3\n\ts_nop 0\n\tglobal_load_lds_dwordx4 %1, %2\n\ts_mov_b32 m0, %0" : "=&s"(keep) : "v"(voff), "s"(base), "s"(lds) : "memory");
}
template <int OFF> __device__ __forceinline__ void gload_nt(v4u& r, const void* base, unsigned voff) {
  if constexpr (MK_VNT) asm volatile("global_load_dwordx4 %0, %1, %2 offset:%3 nt" : "=v"(r) : "v"(voff), "s"(base), "i"(OFF) : "memory");
  else asm volatile("global_load_dwordx4 %0, %1, %2 offset:%3" : "=v"(r) : "v"(voff), "s"(base), "i"(OFF) : "memory");
}
__device__ __forceinline__ void gload(v4u& r, const void* base, unsigned voff) {
  asm volatile("global_load_dwordx4 %0, %1, %2" : "=v"(r) : "v"(voff), "s"(base) : "memory");
}
__device__ __forceinline__ void pin(v4u& r) { asm volatile("" : "+v"(r)); }
__device__ __forceinline__ void dma16_flat(const void* src, unsigned lds) {
  unsigned keep;
  asm volatile("s_mov_b32 %0, m0\n\ts_mov_b32 m0, %2\n\ts_nop 0\n\tglobal_load_lds_dwordx4 %1, off\n\ts_mov_b32 m0, %0" : "=&s"(keep) : "v"(src), "s"(lds) : "memory");
}
template <int N> __device__ __forceinline__ void wait_vm_on(v4u& r) { asm volatile("s_waitcnt vmcnt(%1)" : "+v"(r) : "i"(N) : "memory"); }
__device__ __forceinline__ void sleep_n(int n) { for (int i = 0; i < n; ++i) __builtin_amdgcn_s_sleep(1); }
template <int N> __device__ __forceinline__ void sleep_c() { if constexpr (N > 0) __builtin_amdgcn_s_sleep(N); }

constexpr int SC1 = 16;
typedef __amdgpu_buffer_rsrc_t rsrc_t;
__device__ __forceinline__ v4u ld16(rsrc_t rs, unsigned off) { return __builtin_amdgcn_raw_buffer_load_b128(rs, (int)off, 0, SC1); }
__device__ __forceinline__ v2u ld8(rsrc_t rs, unsigned off) { return __builtin_amdgcn_raw_buffer_load_b64(rs, (int)off, 0, SC1); }
__device__ __forceinline__ void st16(rsrc_t rs, unsigned off, v4u v) { __builtin_amdgcn_raw_buffer_store_b128(v, rs, (int)off, 0, SC1); }
__device__ __forceinline__ void st8(rsrc_t rs, unsigned off, v2u v) { __builtin_amdgcn_raw_buffer_store_b64(v, rs, (int)off, 0, SC1); }
__device__ __forceinline__ void st4(rsrc_t rs, unsigned off, unsigned v) { __builtin_amdgcn_raw_buffer_store_b32(v, rs, (int)off, 0, SC1); }
struct Tag { unsigned lo, hi, f0, f1, f2, f3; };
__device__ __forceinline__ Tag make_tag(unsigned lo, unsigned hi) {
  const u64 m = (((u64)hi << 32) | lo) * 0x9E3779B97F4A7C15ull;
  return Tag{lo, hi, (unsigned)m & 0xfffu, (unsigned)(m >> 12) & 0xfffu, (unsigned)(m >> 24) & 0xfffu, (unsigned)(m >> 36) & 0xfffu};
}
__device__ __forceinline__ v4u pack4(f32x4 v, const Tag& t) {
  return v4u{((asu(v[0]) + 0x800u) & 0xfffff000u) | t.f0, ((asu(v[1]) + 0x800u) & 0xfffff000u) | t.f1, ((asu(v[2]) + 0x800u) & 0xfffff000u) | t.f2, ((asu(v[3]) + 0x800u) & 0xfffff000u) | t.f3};
}
__device__ __forceinline__ bool ok4(v4u v, const Tag& t) { return (((v.x ^ t.f0) | (v.y ^ t.f1) | (v.z ^ t.f2) | (v.w ^ t.f3)) & 0xfffu) == 0u; }
__device__ __forceinline__ f32x4 unpack4(v4u v) { return f32x4{asf(v.x & 0xfffff000u), asf(v.y & 0xfffff000u), asf(v.z & 0xfffff000u), asf(v.w & 0xfffff000u)}; }
__device__ __forceinline__ bool okt(v4u v, const Tag& t) { return v.z == t.lo && v.w == t.hi; }
#define POLL_GUARD(sp) if (++(sp) > SPIN_MAX) break; __builtin_amdgcn_s_sleep(1)

#define STAMP(id) do { } while (0)
#define STAMP_DECL do { } while (0)
#define STAMP_FLUSH(lo, hi) do { } while (0)

__device__ __forceinline__ LAS char* opaque(LAS char* q) { unsigned u = (unsigned)(size_t)q; asm volatile("" : "+v"(u)); return (LAS char*)(size_t)u; }
__device__ __forceinline__ f32x4 gemv32_wave(LAS char* hw, LAS char* act, int lane) {
  const int g = lane >> 4, c = lane & 15;
  LAS char* wa = opaque(hw + (8 * g + (c >> 2)) * 64 + (c & 3) * 8);
  LAS char* aa = opaque(act + (c & 3) * 1024 + g * 16);
  f32x4 acc0 = {0.f, 0.f, 0.f, 0.f}, acc1 = {0.f, 0.f, 0.f, 0.f};
#pragma unroll
  for (int bt = 0; bt < 4; ++bt) {
    v2u a0[4], a1[4], a2[4], a3[4]; v4u bb[4];
#pragma unroll
    for (int k = 0; k < 4; ++k) {
      const int ks = bt * 4 + k;
      a0[k] = tr_read(wa + ks * 2048); a1[k] = tr_read(wa + ks * 2048 + 256); a2[k] = tr_read(wa + ks * 2048 + 32); a3[k] = tr_read(wa + ks * 2048 + 32 + 256);
      bb[k] = *(LAS v4u*)(aa + ks * 64);
    }
    __builtin_amdgcn_sched_barrier(0);
#pragma unroll
    for (int k = 0; k < 4; ++k) {
      acc0 = mfma32(v4u{a0[k].x, a0[k].y, a1[k].x, a1[k].y}, bb[k], acc0);
      acc1 = mfma32(v4u{a2[k].x, a2[k].y, a3[k].x, a3[k].y}, bb[k], acc1);
    }
    __builtin_amdgcn_sched_barrier(0);
  }
  return (c & 4) ? acc1 : acc0;
}

__device__ __forceinline__ float silu(float x) {
  const float t = x * -1.4426950408889634f;
  const float r = __builtin_fmaf(x, -1.4426950408889634f, -t) + x * -1.925963033500011e-08f;
  float e = __builtin_amdgcn_exp2f(t);
  e = __builtin_fmaf(e, r * 0.6931471805599453f, e);
  const float d = 1.f + e, rc = __builtin_amdgcn_rcpf(d), q = x * rc;
  const float s = __builtin_fmaf(__builtin_fmaf(-q, d, x), rc, q);
  return e < 3.0e38f ? s : -0.f;
}
__device__ __forceinline__ void ln_stats(const float (&x)[8], float& mb, float& rden) {
  float s = ((x[0] + x[1]) + (x[2] + x[3])) + ((x[4] + x[5]) + (x[6] + x[7]));
  s = wave_sum(s);
  const float mean = s * (1.f / DM);
  float v = 0.f;
#pragma unroll
  for (int j = 0; j < 8; ++j) { const float d = x[j] - mean; v = __builtin_fmaf(d, d, v); }
  v = wave_sum(v);
  mb = rbf(mean);
  rden = __builtin_amdgcn_rcpf(rbf(__builtin_sqrtf(rbf(rbf(v * (1.f / DM)) + 1e-5f))));
}
__device__ __forceinline__ void rbf2(float& a, float& b) { const unsigned u = pk2(a, b); a = bf_lo(u); b = bf_hi(u); }
__device__ __forceinline__ unsigned modulate2(float x0, float x1, float mb, float rden, float c0, float c1, float s0, float s1) {
  x0 -= mb; x1 -= mb; rbf2(x0, x1);
  x0 *= rden; x1 *= rden; rbf2(x0, x1);
  c0 += 1.f; c1 += 1.f; rbf2(c0, c1);
  x0 *= c0; x1 *= c1; rbf2(x0, x1);
  return pk2(x0 + s0, x1 + s1);
}
__device__ __forceinline__ void unpack8(v4u u, float (&f)[8]) {
  f[0] = bf_lo(u.x); f[1] = bf_hi(u.x); f[2] = bf_lo(u.y); f[3] = bf_hi(u.y); f[4] = bf_lo(u.z); f[5] = bf_hi(u.z); f[6] = bf_lo(u.w); f[7] = bf_hi(u.w);
}
__global__ void __launch_bounds__(NTHR, 1) fused_block(Params p) {
  __shared__ __attribute__((aligned(16))) char smem_[163840];
  LAS char* L = (LAS char*)smem_;
  const int tid = threadIdx.x, lane = tid & 63, w = __builtin_amdgcn_readfirstlane(tid >> 6);
  const int blk = blockIdx.x, xl = blk & 7, bi = blk >> 3;
  const int g = lane >> 4, c = lane & 15;
  const unsigned lds0 = (unsigned)(size_t)L;
  const rsrc_t rs = __builtin_amdgcn_make_buffer_rsrc((void*)p.ws, 0, (int)WS_END, 0x00020000);
  const int chunk = xl * 32 + bi;
  const int rb = bi >> 3, cgr = 8 * xl + (bi & 7);
  const bool ffn = bi < 28;
  const unsigned myb = WS_BLK + blk * BLK_STRIDE;
  volatile LAS unsigned* qflag = (volatile LAS unsigned*)(L + L_FLAG);
  constexpr float SCALE = 0.08838834764831845f, LOG2E = 1.4426950408889634f;
  STAMP_DECL;

  if (w == 8) {
    STAMP(0);
    __builtin_amdgcn_s_setprio(3);
    const bool qrole = bi < 16, m1 = !qrole && (bi < 20 || bi >= 28);
    const int hcol = qrole ? 512 * xl + 32 * bi : m1 ? 128 * xl + 32 * (bi & 3) : 1024 + 256 * xl + 32 * (bi - 20);
    if (MK_ABL & 2) {
      if (!(MK_ABL & 8)) {
        const unsigned rstride = qrole ? 8192u : 6144u;
        const char* wb = (qrole ? (const char*)p.wq : (const char*)p.wmod) + hcol * 2;
        const unsigned vo = (lane >> 2) * rstride + (lane & 3) * 16;
        static_for<0, 32>([&](auto t) { dma16<MK_HNT>(wb + t * 16 * rstride, vo, lds0 + L_HW2 + t * 1024); });
      }
      if (lane == 0) { qflag[0] = 1u; qflag[1] = (MK_ABL & 8) ? 1u : 0u; }
      bar(); bar();
      wait_vm<0>(); qflag[1] = 1u;
      bar(); STAMP(8); STAMP_FLUSH(0, 12); return;
    }
    v2u wv;
    asm volatile("buffer_load_dwordx2 %0, %1, %2, 0 offen sc1" : "=v"(wv) : "v"(WS_W), "s"(rs) : "memory");
    {
      const unsigned rstride = qrole ? 8192u : 6144u;
      const char* wb = (qrole ? (const char*)p.wq : (const char*)p.wmod) + hcol * 2;
      const unsigned vo = (lane >> 2) * rstride + (lane & 3) * 16;
      static_for<0, 32>([&](auto t) { dma16<MK_HNT>(wb + t * 16 * rstride, vo, lds0 + L_HW2 + t * 1024); });
    }
    if (lane == 0) { qflag[0] = 0u; qflag[1] = 0u; }
    bar();
    if (MK_M1HOLD && m1) wait_vm<0>();
    bar();
    wait_vm<0>();
    asm volatile("" : "+v"(wv));
    const u64 tag64 = (((u64)wv.y << 32) | wv.x) + 1ull;
    const Tag tg = make_tag((unsigned)tag64, (unsigned)(tag64 >> 32));
    if (lane == 0) *(LAS v2u*)(L + L_TAG) = v2u{tg.lo, tg.hi};
    STAMP(22);
    if (!qrole) {
      const f32x4 mo = gemv32_wave(L + L_HW2, L + L_ACT, lane);
      unsigned d0 = pk2(mo[0], mo[1]), d1 = pk2(mo[2], mo[3]);
      const int col = hcol + 16 * ((c >> 2) & 1) + 4 * g;
      if ((hcol & ~511) == 512 || (hcol & ~511) == 2048) { d0 = pk2(bf_lo(d0) + 1.f, bf_hi(d0) + 1.f); d1 = pk2(bf_lo(d1) + 1.f, bf_hi(d1) + 1.f); }
      if (c < 8) st16(rs, WS_MT + (c & 3) * 12288 + (col >> 2) * 16, v4u{d0, d1, tg.lo, tg.hi});
      wait_lgkm0();
      qflag[1] = 1u;
    } else {
      sleep_c<MK_DM>();
      v4u sa[4][2], ca[4][2];
      for (unsigned sp = 0;;) {
        bool ok = true;
#pragma unroll
        for (int b = 0; b < 4; ++b)
#pragma unroll
          for (int j = 0; j < 2; ++j) { sa[b][j] = ld16(rs, WS_MT + b * 12288 + lane * 32 + j * 16); ca[b][j] = ld16(rs, WS_MT + b * 12288 + 2048 + lane * 32 + j * 16); }
#pragma unroll
        for (int b = 0; b < 4; ++b)
#pragma unroll
          for (int j = 0; j < 2; ++j) ok = ok && okt(sa[b][j], tg) && okt(ca[b][j], tg);
        if (__all(ok)) break;
        if (++sp > SPIN_MAX) break;
        sleep_c<MK_DP>();
      }
      STAMP(2);
#pragma unroll
      for (int b = 0; b < 4; ++b) {
        float n[8], a[8], sh[8];
        unpack8(*(LAS v4u*)(L + L_XN + b * 1024 + lane * 16), n);
        unpack8(v4u{ca[b][0].x, ca[b][0].y, ca[b][1].x, ca[b][1].y}, a); unpack8(v4u{sa[b][0].x, sa[b][0].y, sa[b][1].x, sa[b][1].y}, sh);
        unsigned h[4];
#pragma unroll
        for (int j = 0; j < 4; ++j) {
          float x0 = n[2 * j] * a[2 * j], x1 = n[2 * j + 1] * a[2 * j + 1]; rbf2(x0, x1);
          h[j] = pk2(x0 + sh[2 * j], x1 + sh[2 * j + 1]);
        }
        *(LAS v4u*)(L + L_ACT + b * 1024 + lane * 16) = v4u{h[0], h[1], h[2], h[3]};
      }
      STAMP(4);
      const f32x4 qo = gemv32_wave(L + L_HW2, L + L_ACT, lane);
      if (c < 8) st16(rs, WS_QT + (c & 3) * 16384 + ((hcol + 16 * (c >> 2) + 4 * g) >> 2) * 16, v4u{pk2(qo[0], qo[1]), pk2(qo[2], qo[3]), tg.lo, tg.hi});
      wait_lgkm0();
      qflag[1] = 1u;
    }
    STAMP(5);
    if (qrole) sleep_c<MK_DQ>(); else sleep_c<MK_DQ2>();
    {
      v4u q0, q1;
      const unsigned qoff = WS_QT + (chunk >> 6) * 16384 + ((chunk >> 3) & 7) * 2048 + lane * 32;
      for (unsigned sp = 0;;) {
        q0 = ld16(rs, qoff); q1 = ld16(rs, qoff + 16);
        if (__all(okt(q0, tg) && okt(q1, tg))) break;
        if (++sp > SPIN_MAX) break;
        sleep_c<MK_DP>();
      }
      *(LAS v4u*)(L + L_QS + lane * 16) = v4u{q0.x, q0.y, q1.x, q1.y};
      wait_lgkm0();
      *qflag = 1u;
    }
    STAMP(6);
    float gav[4], gbv[4], xres[4];
    {
      v4u c1, s2, ga, gb;
      const unsigned go = WS_MT + rb * 12288 + (2 * cgr + (lane & 1)) * 16;
      const unsigned co = WS_MT + g * 12288 + (16 * xl + c) * 16;
      const v2u xr = *(const GAS v2u*)(p.x + rb * DM + 8 * cgr + 4 * (lane & 1));
      for (unsigned sp = 0;;) {
        c1 = ld16(rs, co + 8192); s2 = ld16(rs, co + 6144); ga = ld16(rs, go + 4096); gb = ld16(rs, go + 10240);
        if (__all(okt(c1, tg) && okt(s2, tg) && okt(ga, tg) && okt(gb, tg))) break;
        if (++sp > SPIN_MAX) break;
        sleep_c<MK_DP>();
      }
      *(LAS f32x4*)(L + L_C1 + g * 256 + c * 16) = f32x4{bf_lo(c1.x), bf_hi(c1.x), bf_lo(c1.y), bf_hi(c1.y)};
      *(LAS f32x4*)(L + L_S2 + g * 256 + c * 16) = f32x4{bf_lo(s2.x), bf_hi(s2.x), bf_lo(s2.y), bf_hi(s2.y)};
      gav[0] = bf_lo(ga.x); gav[1] = bf_hi(ga.x); gav[2] = bf_lo(ga.y); gav[3] = bf_hi(ga.y);
      gbv[0] = bf_lo(gb.x); gbv[1] = bf_hi(gb.x); gbv[2] = bf_lo(gb.y); gbv[3] = bf_hi(gb.y);
      xres[0] = bf_lo(xr.x); xres[1] = bf_hi(xr.x); xres[2] = bf_lo(xr.y); xres[3] = bf_hi(xr.y);
    }
    STAMP(7);
    bar();
    STAMP(8);
    if (MK_ABL & 1) { STAMP_FLUSH(0, 12); STAMP_FLUSH(22, 24); return; }
    bar();
    const int pr = lane >> 1;
    const unsigned tcol = (64 * (xl & 1) + 8 * (bi & 7) + 4 * (lane & 1)) * 4 + rb * 512;
    float x1[4];
    sleep_c<MK_D2>();
    {
      const unsigned a0 = WS_BLK + (8 * (4 * (pr >> 3) + (xl >> 1)) + (pr & 7)) * BLK_STRIDE + BLK_AP + tcol, a1 = a0 + 128 * BLK_STRIDE;
      v4u va, vb;
      for (unsigned sp = 0;;) { va = ld16(rs, a0); vb = ld16(rs, a1); if (__all(ok4(va, tg) && ok4(vb, tg))) break; POLL_GUARD(sp); }
      const f32x4 s = unpack4(va) + unpack4(vb);
#pragma unroll
      for (int j = 0; j < 4; ++j) {
        float t = s[j];
        t += dpp<XOR2>(t); t += dpp<ROR4>(t); t += dpp<ROR8>(t); t = x16_sum(t);
        x1[j] = rbf(xres[j] + rbf(gav[j] * rbf(t)));
      }
      if (lane < 2) st16(rs, WS_XT + rb * 2048 + (2 * cgr + lane) * 16, v4u{pk2(x1[0], x1[1]), pk2(x1[2], x1[3]), tg.lo, tg.hi});
    }
    STAMP(10);
    bar();
    bar();
    if (ffn) {
      sleep_c<MK_D4>();
      const int fq = 7 * xl + (bi >> 2);
      const unsigned po = WS_BLK + 8 * (fq >> 1) * BLK_STRIDE + BLK_PP + (lane >> 5) * 1024 + ((lane >> 3) & 3) * 256 + ((fq & 1) * 32 + (lane & 7) * 4) * 4;
      v4u v[8];
      for (unsigned sp = 0;;) {
        bool ok = true;
#pragma unroll
        for (int k = 0; k < 8; ++k) v[k] = ld16(rs, po + k * BLK_STRIDE);
#pragma unroll
        for (int k = 0; k < 8; ++k) ok = ok && ok4(v[k], tg);
        if (__all(ok)) break;
        POLL_GUARD(sp);
      }
      f32x4 s = unpack4(v[0]);
#pragma unroll
      for (int k = 1; k < 8; ++k) s += unpack4(v[k]);
      float gl[4];
#pragma unroll
      for (int j = 0; j < 4; ++j) {
        const float t = rbf(s[j]);
        auto sw = __builtin_amdgcn_permlane32_swap(asu(t), asu(t), false, false);
        const float up = asf(sw[1]);
        const float z = 0.7978845608028654f * (t + 0.044715f * t * t * t);
        const float th = 1.f - 2.f * __builtin_amdgcn_rcpf(1.f + __builtin_amdgcn_exp2f(z * (2.f * LOG2E)));
        gl[j] = rbf(0.5f * t * (1.f + th)) * up;
      }
      if (lane < 32) *(LAS v2u*)(L + L_GB + (lane >> 3) * 64 + (lane & 7) * 8) = v2u{pk2(gl[0], gl[1]), pk2(gl[2], gl[3])};
    }
    STAMP(11);
    bar();
    sleep_c<MK_D5>();
    {
      const unsigned a0 = WS_BLK + (8 * (4 * (pr >> 3) + (xl >> 1)) + (pr & 7)) * BLK_STRIDE + BLK_YP + tcol;
      const bool two = pr < 24;
      const unsigned a1 = two ? a0 + 128 * BLK_STRIDE : a0;
      v4u va, vb;
      for (unsigned sp = 0;;) { va = ld16(rs, a0); vb = ld16(rs, a1); if (__all(ok4(va, tg) && ok4(vb, tg))) break; POLL_GUARD(sp); }
      f32x4 s = unpack4(va);
      if (two) s += unpack4(vb);
      float o[4];
#pragma unroll
      for (int j = 0; j < 4; ++j) {
        float t = s[j];
        t += dpp<XOR2>(t); t += dpp<ROR4>(t); t += dpp<ROR8>(t); t = x16_sum(t);
        o[j] = x1[j] + rbf(gbv[j] * rbf(t));
      }
      if (lane < 2) *(GAS v2u*)(p.out + rb * DM + 8 * cgr + 4 * lane) = v2u{pk2(o[0], o[1]), pk2(o[2], o[3])};
      if (blk == 0 && lane == 0) st8(rs, WS_W, v2u{tg.lo, tg.hi});
    }
    STAMP(9);
    STAMP_FLUSH(0, 12);
    STAMP_FLUSH(22, 24);
    return;
  }

  const char* kb = (const char*)p.kc + (size_t)chunk * 262144 + w * 32768;
  const char* vb_ = (const char*)p.vc + (size_t)chunk * 262144 + w * 32768;
  const unsigned kreg = lds0 + L_K + w * 16384;
  unsigned ko[4];
#pragma unroll
  for (int j = 0; j < 4; ++j) ko[j] = j * 1024 + g * 256 + ((c ^ (4 * j + g)) * 16);
  const unsigned vo = g * 1024 + c * 16;
  constexpr int W = MK_WIN;
  v4u V[20];
  v4u cv;
  gload(cv, (bi < 16 ? p.x : p.c) + (w & 3) * DM, lane * 16);
  bar();
  auto kfill = [&](auto half, auto lo, auto hi, auto first) {
    static_for<lo, hi>([&](auto ii) { constexpr int i = ii; if constexpr (i >= first) wait_vm<W - 1>(); dma16<MK_KNT>(kb + half * 16384 + (i >> 2) * 4096, ko[i & 3], kreg + i * 1024); });
  };
  using I0 = std::integral_constant<int, 0>; using I1 = std::integral_constant<int, 1>; using IW = std::integral_constant<int, W>; using I16 = std::integral_constant<int, 16>;
  if (w != 7) kfill(I0{}, I0{}, IW{}, IW{});
  if (w == 7) wait_vm<0>(); else wait_vm<W>();
  pin(cv);
  if (w < 4) {
    float f[8];
    unpack8(cv, f);
    if (bi < 16) {
      float mb, rden;
      ln_stats(f, mb, rden);
      unsigned n[4];
#pragma unroll
      for (int j = 0; j < 4; ++j) { float x0 = f[2 * j] - mb, x1 = f[2 * j + 1] - mb; rbf2(x0, x1); n[j] = pk2(x0 * rden, x1 * rden); }
      *(LAS v4u*)(L + L_XN + w * 1024 + lane * 16) = v4u{n[0], n[1], n[2], n[3]};
    } else {
#pragma unroll
      for (int j = 0; j < 8; ++j) f[j] = silu(f[j]);
      *(LAS v4u*)(L + L_ACT + w * 1024 + lane * 16) = v4u{pk2(f[0], f[1]), pk2(f[2], f[3]), pk2(f[4], f[5]), pk2(f[6], f[7])};
    }
  }
  bar();
  if (w != 7) kfill(I0{}, IW{}, I16{}, I0{});
  static_for<0, 20>([&](auto jj) { constexpr int j = jj; wait_vm<W - 1>(); gload_nt<(j & 3) * 256>(V[j], vb_ + (j >> 2) * 4096, vo); });
  if (w == 7) {
    for (unsigned sp = 0; qflag[1] == 0u;) { if (++sp > (SPIN_MAX << 4)) break; __builtin_amdgcn_s_sleep(1); }
    asm volatile("" ::: "memory");
    kfill(I0{}, I0{}, I16{}, std::integral_constant<int, MK_W7>{});
  }
  if (w == 0) STAMP(12);
  for (unsigned sp = 0; *qflag == 0u;) { if (++sp > (SPIN_MAX << 4)) break; __builtin_amdgcn_s_sleep(1); }
  if (w == 0) STAMP(13);
  asm volatile("" ::: "memory");
  v4u qf[4];
#pragma unroll
  for (int ks = 0; ks < 4; ++ks) qf[ks] = *(LAS v4u*)(L + L_QS + (c >> 2) * 256 + ks * 64 + g * 16);
  LAS char* kread[4];
#pragma unroll
  for (int ks = 0; ks < 4; ++ks) kread[ks] = L + L_K + w * 16384 + c * 256 + (((4 * ks) ^ (g ^ c)) * 16);
  auto qk = [&](f32x4 (&s)[4]) {
#pragma unroll
    for (int t = 0; t < 4; ++t) {
      f32x4 a = {0.f, 0.f, 0.f, 0.f};
#pragma unroll
      for (int ks = 0; ks < 4; ++ks) a = mfma32(*(LAS v4u*)(kread[ks] + t * 4096), qf[ks], a);
      s[t] = a;
    }
  };
  const int jp = c & 3;
  const unsigned selE = (jp & 1) ? 0x01000c0cu : 0x0c0c0100u, selO = (jp & 1) ? 0x03020c0cu : 0x0c0c0302u, selZ = 0x0c0c0c0cu;
  const unsigned s00 = jp < 2 ? selE : selZ, s01 = jp < 2 ? selZ : selE, s10 = jp < 2 ? selO : selZ, s11 = jp < 2 ? selZ : selO;
  float mrun, lrun = 0.f;
  f32x4 acc1 = {0.f, 0.f, 0.f, 0.f}, acc2 = {0.f, 0.f, 0.f, 0.f};
  unsigned pk[8];
  auto scores = [&](f32x4 (&s)[4]) -> float {
    float mx = -INFINITY;
#pragma unroll
    for (int t = 0; t < 4; ++t) {
      const unsigned u0 = pk2(s[t][0], s[t][1]), u1 = pk2(s[t][2], s[t][3]);
      const unsigned v0 = pk2(bf_lo(u0) * SCALE, bf_hi(u0) * SCALE), v1 = pk2(bf_lo(u1) * SCALE, bf_hi(u1) * SCALE);
      s[t] = f32x4{bf_lo(v0), bf_hi(v0), bf_lo(v1), bf_hi(v1)};
      mx = fmaxf(fmaxf(mx, fmaxf(s[t][0], s[t][1])), fmaxf(s[t][2], s[t][3]));
    }
    return x16_max(mx);
  };
  auto probs = [&](f32x4 (&s)[4], float m) {
#pragma unroll
    for (int t = 0; t < 4; ++t) {
      float e[4];
#pragma unroll
      for (int r = 0; r < 4; ++r) { e[r] = __builtin_amdgcn_exp2f((s[t][r] - m) * LOG2E); lrun += e[r]; }
      pk[2 * t] = pk2(e[0], e[1]); pk[2 * t + 1] = pk2(e[2], e[3]);
    }
  };
  auto pv = [&](unsigned pr, int odd, const v4u& v) {
    const v2u a = {__builtin_amdgcn_perm(pr, pr, odd ? s10 : s00), __builtin_amdgcn_perm(pr, pr, odd ? s11 : s01)};
    acc1 = mfma16(a, v2u{v.x, v.y}, acc1);
    acc2 = mfma16(a, v2u{v.z, v.w}, acc2);
  };

  f32x4 s[4];
  if (w == 7) wait_vm<0>(); else wait_vm<20>();
  qk(s);
  wait_lgkm0();
  __builtin_amdgcn_sched_barrier(0);
  kfill(I1{}, I0{}, IW{}, IW{});
  __builtin_amdgcn_sched_barrier(0);
  mrun = scores(s);
  probs(s, mrun);
  wait_vm<4 + W>();
  static_for<0, 16>([&](auto ii) { constexpr int i = ii; pin(V[i]); });
  __builtin_amdgcn_sched_barrier(0);
  static_for<0, 16>([&](auto ii) { constexpr int i = ii; pv(pk[i >> 1], i & 1, V[i]); });
  __builtin_amdgcn_sched_barrier(0);
  kfill(I1{}, IW{}, I16{}, I0{});
  v4u V2[12];
  static_for<0, W>([&](auto ii) { constexpr int i = ii; wait_vm<W - 1>(); gload_nt<(i & 3) * 256>(V2[i], vb_ + 16384 + 4096 + (i >> 2) * 4096, vo); });
  if (w == 0) STAMP(14);
  wait_vm<W>();
  qk(s);
  wait_lgkm0();
  __builtin_amdgcn_sched_barrier(0);
  static_for<W, W + 2>([&](auto ii) { constexpr int i = ii; gload_nt<(i & 3) * 256>(V2[i], vb_ + 16384 + 4096 + (i >> 2) * 4096, vo); });
  __builtin_amdgcn_sched_barrier(0);
  {
    const float m2 = scores(s);
    const float mn = fmaxf(mrun, m2);
    const float al = __builtin_amdgcn_exp2f((mrun - mn) * LOG2E);
    lrun *= al;
    const float a0 = asf(__builtin_amdgcn_readlane(asu(al), 0)), a1 = asf(__builtin_amdgcn_readlane(asu(al), 4));
    const float a2 = asf(__builtin_amdgcn_readlane(asu(al), 8)), a3 = asf(__builtin_amdgcn_readlane(asu(al), 12));
    const float af = g == 0 ? a0 : g == 1 ? a1 : g == 2 ? a2 : a3;
    acc1 *= af; acc2 *= af;
    mrun = mn;
    probs(s, mrun);
  }
  static_for<0, 4>([&](auto ii) { constexpr int i = ii; pin(V[16 + i]); });
  __builtin_amdgcn_sched_barrier(0);
  static_for<0, 4>([&](auto ii) { constexpr int i = ii; pv(pk[i >> 1], i & 1, V[16 + i]); });
  __builtin_amdgcn_sched_barrier(0);
  static_for<W + 2, 12>([&](auto ii) { constexpr int i = ii; wait_vm<W - 1>(); gload_nt<(i & 3) * 256>(V2[i], vb_ + 16384 + 4096 + (i >> 2) * 4096, vo); });
  constexpr int NTW = (MK_ABL & 4) ? 0 : 5;
  if (!(MK_ABL & 4)) {
    const char* wo_t = (const char*)p.wo + (size_t)(xl * 512 + (bi >> 2) * 64 + 8 * w) * 1024 + (bi & 3) * 256;
    dma16<MK_TNT>(wo_t, vo, kreg + R_WO); dma16<MK_TNT>(wo_t + 4096, vo, kreg + R_WO + 1024);
    const int fb = ffn ? bi : 0;
    const unsigned gvo = (lane >> 3) * 3584 + (lane & 7) * 16;
    dma16<MK_TNT>((const char*)p.wg + (size_t)(64 * xl + 8 * w) * 3584 + fb * 128, gvo, kreg + R_G);
    dma16<MK_TNT>((const char*)p.wu + (size_t)(64 * xl + 8 * w) * 3584 + fb * 128, gvo, kreg + R_U);
    dma16<MK_TNT>((const char*)p.wd + (size_t)(32 * (7 * xl + (fb >> 2)) + 4 * w) * 1024 + (fb & 3) * 256, vo, kreg + R_DN);
  }
  __builtin_amdgcn_sched_barrier(0);
  static_for<0, 12>([&](auto ii) {
    constexpr int i = ii;
    wait_vm_on<11 - i + NTW>(V2[i]);
    __builtin_amdgcn_sched_barrier(0);
    pv(pk[2 + (i >> 1)], i & 1, V2[i]);
    __builtin_amdgcn_sched_barrier(0);
  });
  if (w == 0) STAMP(15);
  {
    const float lw = x16_sum(lrun);
    LAS char* om = L + L_K + w * 16384 + R_OM + g * 256 + c * 16;
    *(LAS f32x4*)(om) = acc1;
    *(LAS f32x4*)(om + 1024 + 128) = acc2;
    if (g == 0 && jp == 0) { ((LAS float*)(L + L_MW))[w * 4 + (c >> 2)] = mrun; ((LAS float*)(L + L_LW))[w * 4 + (c >> 2)] = lw; }
  }
  bar();
  if (MK_ABL & 1) { if (w == 0) STAMP_FLUSH(12, 21); if (acc1[0] == 1.2345f) p.out[0] = 1; return; }
  const v2u tw = *(LAS v2u*)(L + L_TAG);
  const Tag tg = make_tag(tw.x, tw.y);
  if (w >= 4 && w < 6) {
    const int t = tid - 256, h = t >> 5, cc = (t >> 1) & 15, pl = t & 1;
    float mw[8], M = -INFINITY;
#pragma unroll
    for (int i = 0; i < 8; ++i) { mw[i] = ((LAS float*)(L + L_MW))[i * 4 + h]; M = fmaxf(M, mw[i]); }
    f32x4 o = {0.f, 0.f, 0.f, 0.f}; float l = 0.f;
#pragma unroll
    for (int i = 0; i < 8; ++i) {
      const float f = __builtin_amdgcn_exp2f((mw[i] - M) * LOG2E);
      o += f * *(LAS f32x4*)(L + L_K + i * 16384 + R_OM + pl * (1024 + 128) + h * 256 + cc * 16);
      l += f * ((LAS float*)(L + L_LW))[i * 4 + h];
    }
    st16(rs, myb + BLK_OP + (h * 128 + 8 * cc + 4 * pl) * 4, pack4(o, tg));
    if ((t & 31) == 0) st16(rs, myb + BLK_ML + h * 16, v4u{asu(M), asu(l), tg.lo, tg.hi});
  }
  if (w < 4) {
    sleep_c<MK_D1>();
    const int ca_ = 64 * w + 8 * xl + g, cb_ = ca_ + 4, j = bi >> 2;
    const unsigned ba = WS_BLK + (8 * (ca_ & 31) + (ca_ >> 5)) * BLK_STRIDE, bb = WS_BLK + (8 * (cb_ & 31) + (cb_ >> 5)) * BLK_STRIDE;
    const unsigned oo = BLK_OP + (j * 64 + 4 * c) * 4, mo_ = BLK_ML + (j >> 1) * 16;
    v4u va, vb, la, lb;
    for (unsigned sp = 0;;) {
      va = ld16(rs, ba + oo); vb = ld16(rs, bb + oo); la = ld16(rs, ba + mo_); lb = ld16(rs, bb + mo_);
      if (__all(ok4(va, tg) && ok4(vb, tg) && okt(la, tg) && okt(lb, tg))) break;
      POLL_GUARD(sp);
    }
    const float m_a = asf(la.x), m_b = asf(lb.x);
    const float M = x16_max(fmaxf(m_a, m_b));
    const float wa = __builtin_amdgcn_exp2f((m_a - M) * LOG2E), wb = __builtin_amdgcn_exp2f((m_b - M) * LOG2E);
    const float inv = __builtin_amdgcn_rcpf(x16_sum(wa * asf(la.y) + wb * asf(lb.y)));
    const f32x4 t = wa * unpack4(va) + wb * unpack4(vb);
    const float o0 = x16_sum(t[0]) * inv, o1 = x16_sum(t[1]) * inv, o2 = x16_sum(t[2]) * inv, o3 = x16_sum(t[3]) * inv;
    if (g == 0) *(LAS v2u*)(L + L_OB + w * 128 + c * 8) = v2u{pk2(o0, o1), pk2(o2, o3)};
    if (w == 0) STAMP(16);
  }
  if (w == 4 || w == 5) wait_vm<2>(); else wait_vm<0>();
  bar();
  const int q4 = c >> 2, p4 = c & 3;
  v4u wa2[2], ga2[2], da;
  {
    LAS char* wr = opaque(L + L_K + g * 16384 + R_WO + q4 * 256 + w * 32 + p4 * 8);
#pragma unroll
    for (int ks = 0; ks < 2; ++ks) { const v2u a0 = tr_read(wr + ks * 65536), a1 = tr_read(wr + ks * 65536 + 1024); wa2[ks] = v4u{a0.x, a0.y, a1.x, a1.y}; }
  }
  {
    const v4u b0 = *(LAS v4u*)(L + L_OB + (c & 3) * 128 + g * 16), b1 = *(LAS v4u*)(L + L_OB + (c & 3) * 128 + 64 + g * 16);
    f32x4 d = mfma32(wa2[0], b0, f32x4{0.f, 0.f, 0.f, 0.f});
    d = mfma32(wa2[1], b1, d);
    if (c < 4) st16(rs, myb + BLK_AP + c * 512 + (16 * w + 4 * g) * 4, pack4(d, tg));
  }
  if (w == 0) STAMP(17);
  {
    LAS char* gr = opaque(L + L_K + g * 16384 + (w < 4 ? R_G : R_U) + q4 * 128 + (w & 3) * 32 + p4 * 8);
#pragma unroll
    for (int ks = 0; ks < 2; ++ks) { const v2u a0 = tr_read(gr + ks * 65536), a1 = tr_read(gr + ks * 65536 + 512); ga2[ks] = v4u{a0.x, a0.y, a1.x, a1.y}; }
    LAS char* dr = L + L_K + R_DN + q4 * 256 + w * 32 + p4 * 8;
    const v2u a0 = tr_read(dr + (2 * g) * 16384), a1 = tr_read(dr + (2 * g + 1) * 16384);
    da = v4u{a0.x, a0.y, a1.x, a1.y};
  }
  bar();
  if (w < 4 && ffn) {
    sleep_c<MK_D3>();
    v4u x0, x1, xs;
    const unsigned xo = WS_XT + w * 2048;
    for (unsigned sp = 0;;) {
      x0 = ld16(rs, xo + lane * 32); x1 = ld16(rs, xo + lane * 32 + 16); xs = ld16(rs, xo + (16 * xl + (lane >> 2)) * 16);
      if (__all(okt(x0, tg) && okt(x1, tg) && okt(xs, tg))) break;
      POLL_GUARD(sp);
    }
    float xf[8], mb, rden;
    unpack8(v4u{x0.x, x0.y, x1.x, x1.y}, xf);
    ln_stats(xf, mb, rden);
    const unsigned xw = (lane & 2) ? xs.y : xs.x;
    float v = ((lane & 1) ? bf_hi(xw) : bf_lo(xw)) - mb;
    v = rbf(rbf(v) * rden);
    v = rbf(v * ((LAS float*)(L + L_C1))[w * 64 + lane]) + ((LAS float*)(L + L_S2))[w * 64 + lane];
    *(LAS u16*)(L + L_H2 + w * 128 + lane * 2) = (u16)(pk2(v, v) & 0xffffu);
  }
  if (w == 0) STAMP(18);
  bar();
  if (ffn) {
    const v4u b0 = *(LAS v4u*)(L + L_H2 + (c & 3) * 128 + g * 16), b1 = *(LAS v4u*)(L + L_H2 + (c & 3) * 128 + 64 + g * 16);
    f32x4 d = mfma32(ga2[0], b0, f32x4{0.f, 0.f, 0.f, 0.f});
    d = mfma32(ga2[1], b1, d);
    if (c < 4) st16(rs, myb + BLK_PP + (w >> 2) * 1024 + c * 256 + (16 * (w & 3) + 4 * g) * 4, pack4(d, tg));
  }
  if (w == 0) STAMP(19);
  bar();
  if (ffn) {
    const v4u b0 = *(LAS v4u*)(L + L_GB + (c & 3) * 64 + g * 16);
    const f32x4 d = mfma32(da, b0, f32x4{0.f, 0.f, 0.f, 0.f});
    if (c < 4) st16(rs, myb + BLK_YP + c * 512 + (16 * w + 4 * g) * 4, pack4(d, tg));
  }
  if (w == 0) { STAMP(20); STAMP_FLUSH(12, 21); }
}

}

extern "C" void kernel_launch(void* const* d_in, const int* in_sizes, int n_in, void* d_out, int out_size, void* d_ws, size_t ws_size, hipStream_t stream) {
  mk::Params p;
  p.x = (const mk::u16*)d_in[0]; p.kc = (const mk::u16*)d_in[1]; p.vc = (const mk::u16*)d_in[2];
  p.wq = (const mk::u16*)d_in[3]; p.wo = (const mk::u16*)d_in[4];
  p.wg = (const mk::u16*)d_in[6]; p.wu = (const mk::u16*)d_in[7]; p.wd = (const mk::u16*)d_in[8];
  p.c = (const mk::u16*)d_in[9]; p.wmod = (const mk::u16*)d_in[10];
  p.out = (mk::u16*)d_out; p.ws = (char*)d_ws;
  hipLaunchKernelGGL(mk::fused_block, dim3(mk::NBLK), dim3(mk::NTHR), 0, stream, p);
}
```

```cpp
#include <hip/hip_runtime.h>
#include <hip/hip_bf16.h>
#include <stdint.h>
#include <type_traits>

namespace mk {

typedef unsigned short u16;
typedef unsigned long long u64;
typedef unsigned v4u __attribute__((ext_vector_type(4)));
typedef unsigned v2u __attribute__((ext_vector_type(2)));
typedef float f32x4 __attribute__((ext_vector_type(4)));
typedef float f32x2 __attribute__((ext_vector_type(2)));
typedef short s16x4 __attribute__((ext_vector_type(4)));
typedef __bf16 bf16x8 __attribute__((ext_vector_type(8)));
typedef __bf16 bf16x2 __attribute__((ext_vector_type(2)));
#define LAS __attribute__((address_space(3)))
#define GAS __attribute__((address_space(1)))

constexpr int WIN = 3, WIN2 = 3, NEARLY = 4, WIN_LEND = 8;
constexpr int PAUSE_MOD = 100, PAUSE_Q = 35, PAUSE_Q_OTHER = 195, PAUSE_RETRY = 14;
constexpr int PAUSE_O_RETRY = 6;
constexpr int PAUSE_O = 27, PAUSE_A = 24, PAUSE_X = 23, PAUSE_P = 24, PAUSE_Y = 25;

constexpr int DM = 512;
constexpr int NBLK = 256, NTHR = 576;
constexpr unsigned SPIN_MAX = 1u << 17;

constexpr unsigned WS_W = 0;
constexpr unsigned WS_MT = 4096;
constexpr unsigned WS_QT = WS_MT + 49152;
constexpr unsigned WS_XT = WS_QT + 65536;
constexpr unsigned WS_BLK = WS_XT + 8192;
constexpr unsigned BLK_OP = 0;
constexpr unsigned BLK_AP = 2048;
constexpr unsigned BLK_PP = 4096;
constexpr unsigned BLK_YP = BLK_OP;
constexpr unsigned BLK_ML = 6144;
constexpr unsigned BLK_STRIDE = 6272;
constexpr unsigned WS_END = WS_BLK + NBLK * BLK_STRIDE;

constexpr unsigned L_K = 0;
constexpr unsigned L_HW2 = 114688;
constexpr unsigned L_ACT = 131072 + 16384;
constexpr unsigned L_XN = L_ACT + 4096;
constexpr unsigned L_QS = L_XN + 4096;
constexpr unsigned L_C1 = L_QS + 1024;
constexpr unsigned L_S2 = L_C1 + 1024;
constexpr unsigned L_MW = L_S2 + 1024;
constexpr unsigned L_LW = L_MW + 128;
constexpr unsigned L_OB = L_LW + 128;
constexpr unsigned L_H2 = L_OB + 512;
constexpr unsigned L_GB = L_H2 + 512;
constexpr unsigned L_TAG = L_GB + 256;
constexpr unsigned L_FLAG = L_TAG + 16;
constexpr unsigned L_END = L_FLAG + 16;
static_assert(L_END <= 163840, "LDS");
constexpr unsigned R_WO = 0;
constexpr unsigned R_G = 2048;
constexpr unsigned R_U = 3072;
constexpr unsigned R_DN = 4096;
constexpr unsigned R_OM = 8192;

struct Params {
  const u16 *x, *kc, *vc, *wq, *wo, *wg, *wu, *wd, *c, *wmod;
  u16* out;
  char* ws;
};

template <int I, int N, class F> __device__ __forceinline__ void static_for(F&& f) {
  if constexpr (I < N) { f(std::integral_constant<int, I>{}); static_for<I + 1, N>(f); }
}
__device__ __forceinline__ float bf_lo(unsigned u) { return __builtin_bit_cast(float, u << 16); }
__device__ __forceinline__ float bf_hi(unsigned u) { return __builtin_bit_cast(float, u & 0xffff0000u); }
__device__ __forceinline__ unsigned pk2(float a, float b) { f32x2 f = {a, b}; return __builtin_bit_cast(unsigned, __builtin_convertvector(f, bf16x2)); }
__device__ __forceinline__ float rbf(float a) { return bf_lo(pk2(a, a)); }
__device__ __forceinline__ float asf(unsigned u) { return __builtin_bit_cast(float, u); }
__device__ __forceinline__ unsigned asu(float f) { return __builtin_bit_cast(unsigned, f); }
template <int CTRL> __device__ __forceinline__ float dpp(float x) { return asf((unsigned)__builtin_amdgcn_mov_dpp((int)asu(x), CTRL, 0xf, 0xf, true)); }
constexpr int XOR1 = 0xB1, XOR2 = 0x4E, ROR4 = 0x124, ROR8 = 0x128;
__device__ __forceinline__ float x16_max(float x) {
  auto s = __builtin_amdgcn_permlane16_swap(asu(x), asu(x), false, false); x = fmaxf(asf(s[0]), asf(s[1]));
  auto t = __builtin_amdgcn_permlane32_swap(asu(x), asu(x), false, false); return fmaxf(asf(t[0]), asf(t[1]));
}
__device__ __forceinline__ float x16_sum(float x) {
  auto s = __builtin_amdgcn_permlane16_swap(asu(x), asu(x), false, false); x = asf(s[0]) + asf(s[1]);
  auto t = __builtin_amdgcn_permlane32_swap(asu(x), asu(x), false, false); return asf(t[0]) + asf(t[1]);
}
__device__ __forceinline__ float row_sum(float x) {
  x += dpp<XOR1>(x); x += dpp<XOR2>(x); x += dpp<ROR4>(x); x += dpp<ROR8>(x); return x;
}
__device__ __forceinline__ float wave_sum(float x) { return x16_sum(row_sum(x)); }
__device__ __forceinline__ f32x4 mfma32(v4u a, v4u b, f32x4 c) {
  return __builtin_amdgcn_mfma_f32_16x16x32_bf16(__builtin_bit_cast(bf16x8, a), __builtin_bit_cast(bf16x8, b), c, 0, 0, 0);
}
__device__ __forceinline__ f32x4 mfma16(v2u a, v2u b, f32x4 c) {
  return __builtin_amdgcn_mfma_f32_16x16x16bf16_1k(__builtin_bit_cast(s16x4, a), __builtin_bit_cast(s16x4, b), c, 0, 0, 0);
}
__device__ __forceinline__ v2u tr_read(LAS char* p) { return __builtin_bit_cast(v2u, __builtin_amdgcn_ds_read_tr16_b64_v4i16((LAS s16x4*)p)); }
template <int N> __device__ __forceinline__ void wait_vm() { asm volatile("s_waitcnt vmcnt(%0)" ::"i"(N) : "memory"); }
__device__ __forceinline__ void wait_lgkm0() { asm volatile("s_waitcnt lgkmcnt(0)" ::: "memory"); }
__device__ __forceinline__ void bar() { asm volatile("s_waitcnt lgkmcnt(0)\n\ts_barrier" ::: "memory"); }
__device__ __forceinline__ void dma16(const void* base, unsigned voff, unsigned lds) {
  asm volatile("s_mov_b32 m0, %2\n\ts_nop 0\n\tglobal_load_lds_dwordx4 %0, %1 nt" ::"v"(voff), "s"(base), "s"(lds) : "memory");
}
template <int OFF> __device__ __forceinline__ void gload_nt(v4u& r, const void* base, unsigned voff) {
  asm volatile("global_load_dwordx4 %0, %1, %2 offset:%3 nt" : "=v"(r) : "v"(voff), "s"(base), "i"(OFF) : "memory");
}
__device__ __forceinline__ void gload(v4u& r, const void* base, unsigned voff) {
  asm volatile("global_load_dwordx4 %0, %1, %2" : "=v"(r) : "v"(voff), "s"(base) : "memory");
}
__device__ __forceinline__ void pin(v4u& r) { asm volatile("" : "+v"(r)); }
template <int N> __device__ __forceinline__ void wait_vm_on(v4u& r) { asm volatile("s_waitcnt vmcnt(%1)" : "+v"(r) : "i"(N) : "memory"); }
template <int N> __device__ __forceinline__ void sleep_c() {
  if constexpr (N > 120) { __builtin_amdgcn_s_sleep(120); sleep_c<N - 120>(); } else if constexpr (N > 0) __builtin_amdgcn_s_sleep(N);
}

constexpr int SC1 = 16;
typedef __amdgpu_buffer_rsrc_t rsrc_t;
__device__ __forceinline__ v4u ld16(rsrc_t rs, unsigned off) { return __builtin_amdgcn_raw_buffer_load_b128(rs, (int)off, 0, SC1); }
__device__ __forceinline__ void st16(rsrc_t rs, unsigned off, v4u v) { __builtin_amdgcn_raw_buffer_store_b128(v, rs, (int)off, 0, SC1); }
__device__ __forceinline__ void st8(rsrc_t rs, unsigned off, v2u v) { __builtin_amdgcn_raw_buffer_store_b64(v, rs, (int)off, 0, SC1); }
struct Tag { unsigned lo, hi, t0, t1, f0, f1, f2, f3; };
__device__ __forceinline__ Tag make_tag(unsigned lo, unsigned hi) {
  const u64 m = (((u64)hi << 32) | lo) * 0x9E3779B97F4A7C15ull;
  return Tag{lo, hi, (unsigned)m, (unsigned)(m >> 32), (unsigned)m & 0xfffu, (unsigned)(m >> 12) & 0xfffu, (unsigned)(m >> 24) & 0xfffu, (unsigned)(m >> 36) & 0xfffu};
}
__device__ __forceinline__ Tag second_tag(Tag t) { t.f0 ^= 0x5a5u; t.f1 ^= 0x3c3u; t.f2 ^= 0x999u; t.f3 ^= 0x6b6u; return t; }
__device__ __forceinline__ v4u pack4(f32x4 v, const Tag& t) {
  return v4u{((asu(v[0]) + 0x800u) & 0xfffff000u) | t.f0, ((asu(v[1]) + 0x800u) & 0xfffff000u) | t.f1, ((asu(v[2]) + 0x800u) & 0xfffff000u) | t.f2, ((asu(v[3]) + 0x800u) & 0xfffff000u) | t.f3};
}
__device__ __forceinline__ bool ok4(v4u v, const Tag& t) { return (((v.x ^ t.f0) | (v.y ^ t.f1) | (v.z ^ t.f2) | (v.w ^ t.f3)) & 0xfffu) == 0u; }
__device__ __forceinline__ f32x4 unpack4(v4u v) { return f32x4{asf(v.x & 0xfffff000u), asf(v.y & 0xfffff000u), asf(v.z & 0xfffff000u), asf(v.w & 0xfffff000u)}; }
__device__ __forceinline__ v4u packt(unsigned p0, unsigned p1, const Tag& t) { return v4u{p0, t.t0, p1, t.t1}; }
__device__ __forceinline__ bool okt(v4u v, const Tag& t) { return ((v.y ^ t.t0) | (v.w ^ t.t1)) == 0u; }
#define POLL_GUARD(sp) if (++(sp) > SPIN_MAX) break; __builtin_amdgcn_s_sleep(1)

__device__ __forceinline__ LAS char* opaque(LAS char* q) { unsigned u = (unsigned)(size_t)q; asm volatile("" : "+v"(u)); return (LAS char*)(size_t)u; }
__device__ __forceinline__ f32x4 gemv32_wave(LAS char* hw, LAS char* act, int lane) {
  const int g = lane >> 4, c = lane & 15;
  LAS char* wa = opaque(hw + (8 * g + (c >> 2)) * 64 + (c & 3) * 8);
  LAS char* aa = opaque(act + (c & 3) * 1024 + g * 16);
  f32x4 acc0 = {0.f, 0.f, 0.f, 0.f}, acc1 = {0.f, 0.f, 0.f, 0.f};
#pragma unroll
  for (int bt = 0; bt < 4; ++bt) {
    v2u a0[4], a1[4], a2[4], a3[4]; v4u bb[4];
#pragma unroll
    for (int k = 0; k < 4; ++k) {
      const int ks = bt * 4 + k;
      a0[k] = tr_read(wa + ks * 2048); a1[k] = tr_read(wa + ks * 2048 + 256); a2[k] = tr_read(wa + ks * 2048 + 32); a3[k] = tr_read(wa + ks * 2048 + 32 + 256);
      bb[k] = *(LAS v4u*)(aa + ks * 64);
    }
    __builtin_amdgcn_sched_barrier(0);
#pragma unroll
    for (int k = 0; k < 4; ++k) {
      acc0 = mfma32(v4u{a0[k].x, a0[k].y, a1[k].x, a1[k].y}, bb[k], acc0);
      acc1 = mfma32(v4u{a2[k].x, a2[k].y, a3[k].x, a3[k].y}, bb[k], acc1);
    }
    __builtin_amdgcn_sched_barrier(0);
  }
  return (c & 4) ? acc1 : acc0;
}

__device__ __forceinline__ float silu(float x) {
  const float t = x * -1.4426950408889634f;
  const float r = __builtin_fmaf(x, -1.4426950408889634f, -t) + x * -1.925963033500011e-08f;
  float e = __builtin_amdgcn_exp2f(t);
  e = __builtin_fmaf(e, r * 0.6931471805599453f, e);
  const float d = 1.f + e, rc = __builtin_amdgcn_rcpf(d), q = x * rc;
  const float s = __builtin_fmaf(__builtin_fmaf(-q, d, x), rc, q);
  return e < 3.0e38f ? s : -0.f;
}
__device__ __forceinline__ void ln_stats(const float (&x)[8], float& mb, float& rden) {
  float s = ((x[0] + x[1]) + (x[2] + x[3])) + ((x[4] + x[5]) + (x[6] + x[7]));
  s = wave_sum(s);
  const float mean = s * (1.f / DM);
  float v = 0.f;
#pragma unroll
  for (int j = 0; j < 8; ++j) { const float d = x[j] - mean; v = __builtin_fmaf(d, d, v); }
  v = wave_sum(v);
  mb = rbf(mean);
  rden = __builtin_amdgcn_rcpf(rbf(__builtin_sqrtf(rbf(rbf(v * (1.f / DM)) + 1e-5f))));
}
__device__ __forceinline__ void ln_stats_1pass(const float (&x)[8], float& mb, float& rden) {
  const float k = asf(__builtin_amdgcn_readfirstlane(asu(x[0])));
  float s = 0.f, q = 0.f;
#pragma unroll
  for (int j = 0; j < 8; ++j) { const float d = x[j] - k; s += d; q = __builtin_fmaf(d, d, q); }
  s = wave_sum(s) * (1.f / DM); q = wave_sum(q) * (1.f / DM);
  mb = rbf(k + s);
  rden = __builtin_amdgcn_rcpf(rbf(__builtin_amdgcn_sqrtf(rbf(rbf(__builtin_fmaf(-s, s, q)) + 1e-5f))));
}
__device__ __forceinline__ void rbf2(float& a, float& b) { const unsigned u = pk2(a, b); a = bf_lo(u); b = bf_hi(u); }
__device__ __forceinline__ float reduce4_parity(f32x4 s, int lane) {
  const bool b1 = lane & 2, b2 = lane & 4;
  const float k0 = b1 ? s[2] : s[0], k1 = b1 ? s[3] : s[1], x0 = b1 ? s[0] : s[2], x1 = b1 ? s[1] : s[3];
  const float a0 = k0 + dpp<XOR2>(x0), a1 = k1 + dpp<XOR2>(x1);
  const float k = b2 ? a1 : a0, x = b2 ? a0 : a1;
  float t = k + dpp<ROR4>(x);
  t += dpp<ROR8>(t);
  return x16_sum(t);
}
__device__ __forceinline__ v2u gather4_parity(float v) {
  const float v1 = dpp<ROR4>(v), v2 = dpp<XOR2>(v), v3 = dpp<XOR2>(v1);
  return v2u{pk2(v, v1), pk2(v2, v3)};
}
__device__ __forceinline__ void unpack8(v4u u, float (&f)[8]) {
  f[0] = bf_lo(u.x); f[1] = bf_hi(u.x); f[2] = bf_lo(u.y); f[3] = bf_hi(u.y); f[4] = bf_lo(u.z); f[5] = bf_hi(u.z); f[6] = bf_lo(u.w); f[7] = bf_hi(u.w);
}
__global__ void __launch_bounds__(NTHR, 1) fused_block(Params p) {
  __shared__ __attribute__((aligned(16))) char smem_[163840];
  LAS char* L = (LAS char*)smem_;
  const int tid = threadIdx.x, lane = tid & 63, w = __builtin_amdgcn_readfirstlane(tid >> 6);
  const int blk = blockIdx.x, xl = blk & 7, bi = blk >> 3;
  const int g = lane >> 4, c = lane & 15;
  const unsigned lds0 = (unsigned)(size_t)L;
  const rsrc_t rs = __builtin_amdgcn_make_buffer_rsrc((void*)p.ws, 0, (int)WS_END, 0x00020000);
  const int chunk = xl * 32 + bi;
  const int rb = bi >> 3, cgr = 8 * xl + (bi & 7);
  const bool ffn = bi < 28;
  const unsigned myb = WS_BLK + blk * BLK_STRIDE;
  volatile LAS unsigned* qflag = (volatile LAS unsigned*)(L + L_FLAG);
  constexpr float SCALE = 0.08838834764831845f, LOG2E = 1.4426950408889634f;

  if (w == 8) {
    __builtin_amdgcn_s_setprio(3);
    const bool qrole = bi < 16, m1 = !qrole && (bi < 20 || bi >= 28);
    const int hcol = qrole ? 512 * xl + 32 * bi : m1 ? 128 * xl + 32 * (bi & 3) : 1024 + 256 * xl + 32 * (bi - 20);
    v2u wv;
    asm volatile("buffer_load_dwordx2 %0, %1, %2, 0 offen sc1" : "=v"(wv) : "v"(WS_W), "s"(rs) : "memory");
    {
      const unsigned rstride = qrole ? 8192u : 6144u;
      const char* wb = (qrole ? (const char*)p.wq : (const char*)p.wmod) + hcol * 2;
      const unsigned vo = (lane >> 2) * rstride + (lane & 3) * 16;
      static_for<0, 32>([&](auto t) { dma16(wb + t * 16 * rstride, vo, lds0 + L_HW2 + t * 1024); });
    }
    if (lane == 0) { qflag[0] = 0u; qflag[1] = 0u; }
    if (m1) wait_vm<0>();
    bar();
    wait_vm<0>();
    asm volatile("" : "+v"(wv));
    const u64 tag64 = (((u64)wv.y << 32) | wv.x) + 1ull;
    const Tag tg = make_tag((unsigned)tag64, (unsigned)(tag64 >> 32));
    if (lane == 0) *(LAS v2u*)(L + L_TAG) = v2u{tg.lo, tg.hi};
    if (!qrole) {
      const f32x4 mo = gemv32_wave(L + L_HW2, L + L_ACT, lane);
      unsigned d0 = pk2(mo[0], mo[1]), d1 = pk2(mo[2], mo[3]);
      const int col = hcol + 16 * ((c >> 2) & 1) + 4 * g;
      if ((hcol & ~511) == 512 || (hcol & ~511) == 2048) { d0 = pk2(bf_lo(d0) + 1.f, bf_hi(d0) + 1.f); d1 = pk2(bf_lo(d1) + 1.f, bf_hi(d1) + 1.f); }
      if (c < 8) st16(rs, WS_MT + (c & 3) * 12288 + (col >> 2) * 16, packt(d0, d1, tg));
      wait_lgkm0();
      qflag[1] = 1u;
    } else {
      sleep_c<PAUSE_MOD>();
      v4u sa[4][2], ca[4][2];
      for (unsigned sp = 0;;) {
        bool ok = true;
#pragma unroll
        for (int b = 0; b < 4; ++b)
#pragma unroll
          for (int j = 0; j < 2; ++j) { sa[b][j] = ld16(rs, WS_MT + b * 12288 + lane * 32 + j * 16); ca[b][j] = ld16(rs, WS_MT + b * 12288 + 2048 + lane * 32 + j * 16); }
#pragma unroll
        for (int b = 0; b < 4; ++b)
#pragma unroll
          for (int j = 0; j < 2; ++j) ok = ok & okt(sa[b][j], tg) & okt(ca[b][j], tg);
        if (__all(ok)) break;
        if (++sp > SPIN_MAX) break;
        sleep_c<PAUSE_RETRY>();
      }
#pragma unroll
      for (int b = 0; b < 4; ++b) {
        float n[8], a[8], sh[8];
        unpack8(*(LAS v4u*)(L + L_XN + b * 1024 + lane * 16), n);
        unpack8(v4u{ca[b][0].x, ca[b][0].z, ca[b][1].x, ca[b][1].z}, a); unpack8(v4u{sa[b][0].x, sa[b][0].z, sa[b][1].x, sa[b][1].z}, sh);
        unsigned h[4];
#pragma unroll
        for (int j = 0; j < 4; ++j) {
          float x0 = n[2 * j] * a[2 * j], x1 = n[2 * j + 1] * a[2 * j + 1]; rbf2(x0, x1);
          h[j] = pk2(x0 + sh[2 * j], x1 + sh[2 * j + 1]);
        }
        *(LAS v4u*)(L + L_ACT + b * 1024 + lane * 16) = v4u{h[0], h[1], h[2], h[3]};
      }
      const f32x4 qo = gemv32_wave(L + L_HW2, L + L_ACT, lane);
      if (c < 8) st16(rs, WS_QT + (c & 3) * 16384 + ((hcol + 16 * (c >> 2) + 4 * g) >> 2) * 16, packt(pk2(qo[0], qo[1]), pk2(qo[2], qo[3]), tg));
      wait_lgkm0();
      qflag[1] = 1u;
    }
    if (qrole) sleep_c<PAUSE_Q>(); else sleep_c<PAUSE_Q_OTHER>();
    {
      v4u q0, q1;
      const unsigned qoff = WS_QT + (chunk >> 6) * 16384 + ((chunk >> 3) & 7) * 2048 + lane * 32;
      for (unsigned sp = 0;;) {
        q0 = ld16(rs, qoff); q1 = ld16(rs, qoff + 16);
        if (__all(okt(q0, tg) & okt(q1, tg))) break;
        if (++sp > SPIN_MAX) break;
        sleep_c<PAUSE_RETRY>();
      }
      *(LAS v4u*)(L + L_QS + lane * 16) = v4u{q0.x, q0.z, q1.x, q1.z};
      wait_lgkm0();
      *qflag = 1u;
    }
    float gav, gbv, xres;
    {
      v4u c1, s2, ga, gb;
      const unsigned go = WS_MT + rb * 12288 + (2 * cgr + (lane & 1)) * 16;
      const unsigned co = WS_MT + g * 12288 + (16 * xl + c) * 16;
      const v2u xr = *(const GAS v2u*)(p.x + rb * DM + 8 * cgr + 4 * (lane & 1));
      for (unsigned sp = 0;;) {
        c1 = ld16(rs, co + 8192); s2 = ld16(rs, co + 6144); ga = ld16(rs, go + 4096); gb = ld16(rs, go + 10240);
        if (__all(okt(c1, tg) & okt(s2, tg) & okt(ga, tg) & okt(gb, tg))) break;
        if (++sp > SPIN_MAX) break;
        sleep_c<PAUSE_RETRY>();
      }
      *(LAS f32x4*)(L + L_C1 + g * 256 + c * 16) = f32x4{bf_lo(c1.x), bf_hi(c1.x), bf_lo(c1.z), bf_hi(c1.z)};
      *(LAS f32x4*)(L + L_S2 + g * 256 + c * 16) = f32x4{bf_lo(s2.x), bf_hi(s2.x), bf_lo(s2.z), bf_hi(s2.z)};
      auto pick = [&](unsigned w0, unsigned w1) { const unsigned u = (lane & 2) ? w1 : w0; return (lane & 4) ? bf_hi(u) : bf_lo(u); };
      gav = pick(ga.x, ga.z); gbv = pick(gb.x, gb.z); xres = pick(xr.x, xr.y);
    }
    bar();
    bar();
    const int pr = lane >> 1;
    const unsigned tcol = (64 * (xl & 1) + 8 * (bi & 7) + 4 * (lane & 1)) * 4 + rb * 512;
    float x1;
    sleep_c<PAUSE_A>();
    {
      const unsigned a0 = WS_BLK + (8 * (4 * (pr >> 3) + (xl >> 1)) + (pr & 7)) * BLK_STRIDE + BLK_AP + tcol, a1 = a0 + 128 * BLK_STRIDE;
      v4u va, vb;
      for (unsigned sp = 0;;) { va = ld16(rs, a0); vb = ld16(rs, a1); if (__all(ok4(va, tg) & ok4(vb, tg))) break; POLL_GUARD(sp); }
      const float t = reduce4_parity(unpack4(va) + unpack4(vb), lane);
      x1 = rbf(xres + rbf(gav * rbf(t)));
      const v2u xp = gather4_parity(x1);
      if (lane < 2) st16(rs, WS_XT + rb * 2048 + (2 * cgr + lane) * 16, packt(xp.x, xp.y, tg));
    }
    bar();
    bar();
    if (ffn) {
      sleep_c<PAUSE_P>();
      const int fq = 7 * xl + (bi >> 2);
      const unsigned po = WS_BLK + 8 * (fq >> 1) * BLK_STRIDE + BLK_PP + (lane >> 5) * 1024 + ((lane >> 3) & 3) * 256 + ((fq & 1) * 32 + (lane & 7) * 4) * 4;
      v4u v[8];
      for (unsigned sp = 0;;) {
        bool ok = true;
#pragma unroll
        for (int k = 0; k < 8; ++k) v[k] = ld16(rs, po + k * BLK_STRIDE);
#pragma unroll
        for (int k = 0; k < 8; ++k) ok = ok & ok4(v[k], tg);
        if (__all(ok)) break;
        POLL_GUARD(sp);
      }
      f32x4 s = unpack4(v[0]);
#pragma unroll
      for (int k = 1; k < 8; ++k) s += unpack4(v[k]);
      float gl[4];
#pragma unroll
      for (int j = 0; j < 4; ++j) {
        const float t = rbf(s[j]);
        auto sw = __builtin_amdgcn_permlane32_swap(asu(t), asu(t), false, false);
        const float up = asf(sw[1]);
        const float z = 0.7978845608028654f * (t + 0.044715f * t * t * t);
        const float th = 1.f - 2.f * __builtin_amdgcn_rcpf(1.f + __builtin_amdgcn_exp2f(z * (2.f * LOG2E)));
        gl[j] = rbf(0.5f * t * (1.f + th)) * up;
      }
      if (lane < 32) *(LAS v2u*)(L + L_GB + (lane >> 3) * 64 + (lane & 7) * 8) = v2u{pk2(gl[0], gl[1]), pk2(gl[2], gl[3])};
    }
    bar();
    sleep_c<PAUSE_Y>();
    {
      const unsigned a0 = WS_BLK + (8 * (4 * (pr >> 3) + (xl >> 1)) + (pr & 7)) * BLK_STRIDE + BLK_YP + tcol;
      const bool two = pr < 24;
      const unsigned a1 = two ? a0 + 128 * BLK_STRIDE : a0;
      v4u va, vb;
      const Tag t2 = second_tag(tg);
      for (unsigned sp = 0;;) { va = ld16(rs, a0); vb = ld16(rs, a1); if (__all(ok4(va, t2) & ok4(vb, t2))) break; POLL_GUARD(sp); }
      f32x4 s = unpack4(va);
      if (two) s += unpack4(vb);
      const float t = reduce4_parity(s, lane);
      const v2u ov = gather4_parity(x1 + rbf(gbv * rbf(t)));
      if (lane < 2) *(GAS v2u*)(p.out + rb * DM + 8 * cgr + 4 * lane) = ov;
      if (blk == 0 && lane == 0) st8(rs, WS_W, v2u{tg.lo, tg.hi});
    }
    return;
  }

  const char* kb = (const char*)p.kc + (size_t)chunk * 262144 + w * 4096;
  const char* vb_ = (const char*)p.vc + (size_t)chunk * 262144 + w * 4096;
  const unsigned kreg = lds0 + L_K + w * 16384;
  unsigned ko[4];
#pragma unroll
  for (int j = 0; j < 4; ++j) ko[j] = j * 1024 + g * 256 + ((c ^ (4 * j + g)) * 16);
  const unsigned vo = g * 1024 + c * 16;
  constexpr int W = WIN;
  constexpr int NA = NEARLY, NB2 = 16 - NA;
  v4u V[16 + NA];
  v4u cv;
  gload(cv, (bi < 16 ? p.x : p.c) + (w & 3) * DM, w < 4 ? lane * 16 : 0);
  auto kfill = [&](auto half, auto lo, auto hi, auto first) {
    static_for<lo, hi>([&](auto ii) { constexpr int i = ii; if constexpr (i >= first) wait_vm<W - 1>(); dma16(kb + half * 131072 + (i >> 2) * 32768, ko[i & 3], kreg + i * 1024); });
  };
  using I0 = std::integral_constant<int, 0>; using I1 = std::integral_constant<int, 1>; using I16 = std::integral_constant<int, 16>;
  constexpr int B0 = W;
  static_for<0, B0>([&](auto jj) { constexpr int j = jj; gload_nt<(j & 3) * 256>(V[j], vb_ + (j >> 2) * 32768, vo); });
  wait_vm_on<B0>(cv);
  if (w < 4) {
    float f[8];
    unpack8(cv, f);
    if (bi < 16) {
      float mb, rden;
      ln_stats(f, mb, rden);
      unsigned n[4];
#pragma unroll
      for (int j = 0; j < 4; ++j) { float x0 = f[2 * j] - mb, x1 = f[2 * j + 1] - mb; rbf2(x0, x1); n[j] = pk2(x0 * rden, x1 * rden); }
      *(LAS v4u*)(L + L_XN + w * 1024 + lane * 16) = v4u{n[0], n[1], n[2], n[3]};
    } else {
#pragma unroll
      for (int j = 0; j < 8; ++j) f[j] = silu(f[j]);
      *(LAS v4u*)(L + L_ACT + w * 1024 + lane * 16) = v4u{pk2(f[0], f[1]), pk2(f[2], f[3]), pk2(f[4], f[5]), pk2(f[6], f[7])};
    }
  }
  bar();
  const bool lend = w == 7;
  if (!lend) kfill(I0{}, I0{}, I16{}, I0{});
  static_for<B0, 16 + NA>([&](auto jj) { constexpr int j = jj; wait_vm<W - 1>(); gload_nt<(j & 3) * 256>(V[j], vb_ + (j >> 2) * 32768, vo); });
  if (lend) {
    for (unsigned sp = 0; qflag[1] == 0u;) { if (++sp > (SPIN_MAX << 4)) break; __builtin_amdgcn_s_sleep(1); }
    asm volatile("" ::: "memory");
    kfill(I0{}, I0{}, I16{}, std::integral_constant<int, WIN_LEND>{});
  }
  for (unsigned sp = 0; *qflag == 0u;) { if (++sp > (SPIN_MAX << 4)) break; __builtin_amdgcn_s_sleep(1); }
  asm volatile("" ::: "memory");
  const v2u tw = *(LAS v2u*)(L + L_TAG);
  const Tag tg = make_tag(__builtin_amdgcn_readfirstlane(tw.x), __builtin_amdgcn_readfirstlane(tw.y));
  v4u qf[4];
#pragma unroll
  for (int ks = 0; ks < 4; ++ks) qf[ks] = *(LAS v4u*)(L + L_QS + (c >> 2) * 256 + ks * 64 + g * 16);
  LAS char* kread[4];
#pragma unroll
  for (int ks = 0; ks < 4; ++ks) kread[ks] = L + L_K + w * 16384 + c * 256 + (((4 * ks) ^ (g ^ c)) * 16);
  auto qk = [&](f32x4 (&s)[4]) {
#pragma unroll
    for (int t = 0; t < 4; ++t) {
      f32x4 a = {0.f, 0.f, 0.f, 0.f};
#pragma unroll
      for (int ks = 0; ks < 4; ++ks) a = mfma32(*(LAS v4u*)(kread[ks] + t * 4096), qf[ks], a);
      s[t] = a;
    }
  };
  const int jp = c & 3;
  const unsigned selE = (jp & 1) ? 0x01000c0cu : 0x0c0c0100u, selO = (jp & 1) ? 0x03020c0cu : 0x0c0c0302u, selZ = 0x0c0c0c0cu;
  const unsigned s00 = jp < 2 ? selE : selZ, s01 = jp < 2 ? selZ : selE, s10 = jp < 2 ? selO : selZ, s11 = jp < 2 ? selZ : selO;
  float mrun, lrun = 0.f;
  f32x4 acc1 = {0.f, 0.f, 0.f, 0.f}, acc2 = {0.f, 0.f, 0.f, 0.f};
  unsigned pk[8];
  auto scores = [&](f32x4 (&s)[4]) -> float {
    float mx = -INFINITY;
#pragma unroll
    for (int t = 0; t < 4; ++t) {
      const unsigned u0 = pk2(s[t][0], s[t][1]), u1 = pk2(s[t][2], s[t][3]);
      const unsigned v0 = pk2(bf_lo(u0) * SCALE, bf_hi(u0) * SCALE), v1 = pk2(bf_lo(u1) * SCALE, bf_hi(u1) * SCALE);
      s[t] = f32x4{bf_lo(v0), bf_hi(v0), bf_lo(v1), bf_hi(v1)};
      mx = fmaxf(fmaxf(mx, fmaxf(s[t][0], s[t][1])), fmaxf(s[t][2], s[t][3]));
    }
    return x16_max(mx);
  };
  auto probs = [&](f32x4 (&s)[4], float m) {
#pragma unroll
    for (int t = 0; t < 4; ++t) {
      float e[4];
#pragma unroll
      for (int r = 0; r < 4; ++r) { e[r] = __builtin_amdgcn_exp2f((s[t][r] - m) * LOG2E); lrun += e[r]; }
      pk[2 * t] = pk2(e[0], e[1]); pk[2 * t + 1] = pk2(e[2], e[3]);
    }
  };
  auto pv = [&](unsigned pr, int odd, const v4u& v) {
    const v2u a = {__builtin_amdgcn_perm(pr, pr, odd ? s10 : s00), __builtin_amdgcn_perm(pr, pr, odd ? s11 : s01)};
    acc1 = mfma16(a, v2u{v.x, v.y}, acc1);
    acc2 = mfma16(a, v2u{v.z, v.w}, acc2);
  };

  constexpr int P = WIN2, NV = P < NB2 ? P : NB2, NV2 = NV + 2 < NB2 ? NV + 2 : NB2;
  auto k2fill = [&](auto lo, auto hi, auto thr) {
    static_for<lo, hi>([&](auto ii) { constexpr int i = ii; if constexpr (thr) wait_vm<P - 1>(); dma16(kb + 131072 + (i >> 2) * 32768, ko[i & 3], kreg + i * 1024); });
  };
  v4u V2[NB2];
  auto v2load = [&](auto lo, auto hi, auto thr) {
    static_for<lo, hi>([&](auto ii) { constexpr int i = ii; if constexpr (thr) wait_vm<P - 1>(); gload_nt<((NA + i) & 3) * 256>(V2[i], vb_ + 131072 + ((NA + i) >> 2) * 32768, vo); });
  };
  using IP = std::integral_constant<int, P>; using INV = std::integral_constant<int, NV>; using INV2 = std::integral_constant<int, NV2>; using I12 = std::integral_constant<int, NB2>;
  f32x4 s[4];
  if (lend) wait_vm<0>(); else wait_vm<16 + NA - B0>();
  qk(s);
  wait_lgkm0();
  __builtin_amdgcn_sched_barrier(0);
  k2fill(I0{}, IP{}, I0{});
  __builtin_amdgcn_sched_barrier(0);
  mrun = scores(s);
  probs(s, mrun);
  wait_vm<NA + P>();
  static_for<0, 16>([&](auto ii) { constexpr int i = ii; pin(V[i]); });
  __builtin_amdgcn_sched_barrier(0);
  static_for<0, 16>([&](auto ii) { constexpr int i = ii; pv(pk[i >> 1], i & 1, V[i]); });
  __builtin_amdgcn_sched_barrier(0);
  k2fill(IP{}, I16{}, I1{});
  v2load(I0{}, INV{}, I1{});
  wait_vm<NV>();
  qk(s);
  wait_lgkm0();
  __builtin_amdgcn_sched_barrier(0);
  v2load(INV{}, INV2{}, I0{});
  __builtin_amdgcn_sched_barrier(0);
  {
    const float m2 = scores(s);
    const float mn = fmaxf(mrun, m2);
    const float al = __builtin_amdgcn_exp2f((mrun - mn) * LOG2E);
    lrun *= al;
    const float a0 = asf(__builtin_amdgcn_readlane(asu(al), 0)), a1 = asf(__builtin_amdgcn_readlane(asu(al), 4));
    const float a2 = asf(__builtin_amdgcn_readlane(asu(al), 8)), a3 = asf(__builtin_amdgcn_readlane(asu(al), 12));
    const float af = g == 0 ? a0 : g == 1 ? a1 : g == 2 ? a2 : a3;
    acc1 *= af; acc2 *= af;
    mrun = mn;
    probs(s, mrun);
  }
  static_for<0, NA>([&](auto ii) { constexpr int i = ii; pin(V[16 + i]); });
  __builtin_amdgcn_sched_barrier(0);
  static_for<0, NA>([&](auto ii) { constexpr int i = ii; pv(pk[i >> 1], i & 1, V[16 + i]); });
  __builtin_amdgcn_sched_barrier(0);
  v2load(INV2{}, I12{}, I1{});
  constexpr int NTW = 2, NLATE = 3;
  {
    const char* wo_t = (const char*)p.wo + (size_t)(xl * 512 + (bi >> 2) * 64 + 8 * w) * 1024 + (bi & 3) * 256;
    dma16(wo_t, vo, kreg + R_WO); dma16(wo_t + 4096, vo, kreg + R_WO + 1024);
  }
  __builtin_amdgcn_sched_barrier(0);
  static_for<0, NB2>([&](auto ii) {
    constexpr int i = ii;
    wait_vm_on<NB2 - 1 - i + NTW>(V2[i]);
    __builtin_amdgcn_sched_barrier(0);
    pv(pk[(NA + i) >> 1], (NA + i) & 1, V2[i]);
    __builtin_amdgcn_sched_barrier(0);
  });
  {
    const int fb = ffn ? bi : 0;
    const unsigned gvo = (lane >> 3) * 3584 + (lane & 7) * 16;
    dma16((const char*)p.wg + (size_t)(64 * xl + 8 * w) * 3584 + fb * 128, gvo, kreg + R_G);
    dma16((const char*)p.wu + (size_t)(64 * xl + 8 * w) * 3584 + fb * 128, gvo, kreg + R_U);
    dma16((const char*)p.wd + (size_t)(32 * (7 * xl + (fb >> 2)) + 4 * w) * 1024 + (fb & 3) * 256, vo, kreg + R_DN);
  }
  {
    const float lw = x16_sum(lrun);
    LAS char* om = L + L_K + w * 16384 + R_OM + g * 256 + c * 16;
    *(LAS f32x4*)(om) = acc1;
    *(LAS f32x4*)(om + 1024 + 128) = acc2;
    if (g == 0 && jp == 0) { ((LAS float*)(L + L_MW))[w * 4 + (c >> 2)] = mrun; ((LAS float*)(L + L_LW))[w * 4 + (c >> 2)] = lw; }
  }
  wait_vm<NLATE>();
  bar();
  const int q4 = c >> 2, p4 = c & 3;
  v4u wa2[2];
  {
    LAS char* wr = opaque(L + L_K + g * 16384 + R_WO + q4 * 256 + w * 32 + p4 * 8);
    LAS char* wr1 = opaque(wr + 65536);
    const v2u a0 = tr_read(wr), a1 = tr_read(wr + 1024), a2 = tr_read(wr1), a3 = tr_read(wr1 + 1024);
    wa2[0] = v4u{a0.x, a0.y, a1.x, a1.y}; wa2[1] = v4u{a2.x, a2.y, a3.x, a3.y};
  }
  if (w >= 4 && w < 6) {
    const int t = tid - 256, h = t >> 5, cc = (t >> 1) & 15, pl = t & 1;
    float mw[8], M = -INFINITY;
#pragma unroll
    for (int i = 0; i < 8; ++i) { mw[i] = ((LAS float*)(L + L_MW))[i * 4 + h]; M = fmaxf(M, mw[i]); }
    f32x4 o = {0.f, 0.f, 0.f, 0.f}; float l = 0.f;
#pragma unroll
    for (int i = 0; i < 8; ++i) {
      const float f = __builtin_amdgcn_exp2f((mw[i] - M) * LOG2E);
      o += f * *(LAS f32x4*)(L + L_K + i * 16384 + R_OM + pl * (1024 + 128) + h * 256 + cc * 16);
      l += f * ((LAS float*)(L + L_LW))[i * 4 + h];
    }
    st16(rs, myb + BLK_OP + (h * 128 + 8 * cc + 4 * pl) * 4, pack4(o, tg));
    if ((t & 31) == 0) st16(rs, myb + BLK_ML + h * 16, packt(asu(M), asu(l), tg));
  }
  if (w < 4) {
    sleep_c<PAUSE_O>();
    const int ca_ = 64 * w + 8 * xl + g, cb_ = ca_ + 4, j = bi >> 2;
    const unsigned ba = WS_BLK + (8 * (ca_ & 31) + (ca_ >> 5)) * BLK_STRIDE, bb = WS_BLK + (8 * (cb_ & 31) + (cb_ >> 5)) * BLK_STRIDE;
    const unsigned oo = BLK_OP + (j * 64 + 4 * c) * 4, mo_ = BLK_ML + (j >> 1) * 16;
    v4u va, vb, la, lb;
    for (unsigned sp = 0;;) {
      va = ld16(rs, ba + oo); vb = ld16(rs, bb + oo); la = ld16(rs, ba + mo_); lb = ld16(rs, bb + mo_);
      if (__all(ok4(va, tg) & ok4(vb, tg) & okt(la, tg) & okt(lb, tg))) break;
      if (++sp > SPIN_MAX) break;
      sleep_c<PAUSE_O_RETRY>();
    }
    const float m_a = asf(la.x), m_b = asf(lb.x);
    const float M = x16_max(fmaxf(m_a, m_b));
    const float wa = __builtin_amdgcn_exp2f((m_a - M) * LOG2E), wb = __builtin_amdgcn_exp2f((m_b - M) * LOG2E);
    const float inv = __builtin_amdgcn_rcpf(x16_sum(wa * asf(la.z) + wb * asf(lb.z)));
    const f32x4 t = wa * unpack4(va) + wb * unpack4(vb);
    const float o0 = x16_sum(t[0]) * inv, o1 = x16_sum(t[1]) * inv, o2 = x16_sum(t[2]) * inv, o3 = x16_sum(t[3]) * inv;
    if (g == 0) *(LAS v2u*)(L + L_OB + w * 128 + c * 8) = v2u{pk2(o0, o1), pk2(o2, o3)};
  }
  if (w == 4 || w == 5) wait_vm<2>(); else wait_vm<0>();
  bar();
  v4u ga2[2], da;
  {
    const v4u b0 = *(LAS v4u*)(L + L_OB + (c & 3) * 128 + g * 16), b1 = *(LAS v4u*)(L + L_OB + (c & 3) * 128 + 64 + g * 16);
    f32x4 d = mfma32(wa2[0], b0, f32x4{0.f, 0.f, 0.f, 0.f});
    d = mfma32(wa2[1], b1, d);
    if (c < 4) st16(rs, myb + BLK_AP + c * 512 + (16 * w + 4 * g) * 4, pack4(d, tg));
  }
  {
    LAS char* gr = opaque(L + L_K + g * 16384 + (w < 4 ? R_G : R_U) + q4 * 128 + (w & 3) * 32 + p4 * 8);
#pragma unroll
    for (int ks = 0; ks < 2; ++ks) { const v2u a0 = tr_read(gr + ks * 65536), a1 = tr_read(gr + ks * 65536 + 512); ga2[ks] = v4u{a0.x, a0.y, a1.x, a1.y}; }
    LAS char* dr = L + L_K + R_DN + q4 * 256 + w * 32 + p4 * 8;
    const v2u a0 = tr_read(dr + (2 * g) * 16384), a1 = tr_read(dr + (2 * g + 1) * 16384);
    da = v4u{a0.x, a0.y, a1.x, a1.y};
  }
  bar();
  if (w < 4 && ffn) {
    sleep_c<PAUSE_X>();
    v4u x0, x1, xs;
    const unsigned xo = WS_XT + w * 2048;
    for (unsigned sp = 0;;) {
      x0 = ld16(rs, xo + lane * 32); x1 = ld16(rs, xo + lane * 32 + 16); xs = ld16(rs, xo + (16 * xl + (lane >> 2)) * 16);
      if (__all(okt(x0, tg) & okt(x1, tg) & okt(xs, tg))) break;
      POLL_GUARD(sp);
    }
    float xf[8], mb, rden;
    unpack8(v4u{x0.x, x0.z, x1.x, x1.z}, xf);
    ln_stats_1pass(xf, mb, rden);
    const unsigned xw = (lane & 2) ? xs.z : xs.x;
    float v = ((lane & 1) ? bf_hi(xw) : bf_lo(xw)) - mb;
    v = rbf(rbf(v) * rden);
    v = rbf(v * ((LAS float*)(L + L_C1))[w * 64 + lane]) + ((LAS float*)(L + L_S2))[w * 64 + lane];
    *(LAS u16*)(L + L_H2 + w * 128 + lane * 2) = (u16)(pk2(v, v) & 0xffffu);
  }
  bar();
  if (ffn) {
    const v4u b0 = *(LAS v4u*)(L + L_H2 + (c & 3) * 128 + g * 16), b1 = *(LAS v4u*)(L + L_H2 + (c & 3) * 128 + 64 + g * 16);
    f32x4 d = mfma32(ga2[0], b0, f32x4{0.f, 0.f, 0.f, 0.f});
    d = mfma32(ga2[1], b1, d);
    if (c < 4) st16(rs, myb + BLK_PP + (w >> 2) * 1024 + c * 256 + (16 * (w & 3) + 4 * g) * 4, pack4(d, tg));
  }
  bar();
  if (ffn) {
    const v4u b0 = *(LAS v4u*)(L + L_GB + (c & 3) * 64 + g * 16);
    const f32x4 d = mfma32(da, b0, f32x4{0.f, 0.f, 0.f, 0.f});
    if (c < 4) st16(rs, myb + BLK_YP + c * 512 + (16 * w + 4 * g) * 4, pack4(d, second_tag(tg)));
  }
}

}

extern "C" void kernel_launch(void* const* d_in, const int* in_sizes, int n_in, void* d_out, int out_size, void* d_ws, size_t ws_size, hipStream_t stream) {
  mk::Params p;
  p.x = (const mk::u16*)d_in[0]; p.kc = (const mk::u16*)d_in[1]; p.vc = (const mk::u16*)d_in[2];
  p.wq = (const mk::u16*)d_in[3]; p.wo = (const mk::u16*)d_in[4];
  p.wg = (const mk::u16*)d_in[6]; p.wu = (const mk::u16*)d_in[7]; p.wd = (const mk::u16*)d_in[8];
  p.c = (const mk::u16*)d_in[9]; p.wmod = (const mk::u16*)d_in[10];
  p.out = (mk::u16*)d_out; p.ws = (char*)d_ws;
  hipLaunchKernelGGL(mk::fused_block, dim3(mk::NBLK), dim3(mk::NTHR), 0, stream, p);
}
```
